```python
import torch

def _softmax(x, axis=-1):
    return torch.softmax(x, dim=axis)
def _gelu(x):
    return torch.nn.functional.gelu(x, approximate="tanh")
def _silu(x):
    return torch.nn.functional.silu(x)
def _sigmoid(x):
    return torch.sigmoid(x)
def _layernorm(x, axis=-1, eps=1e-5):
    m = x.mean(dim=axis, keepdim=True)
    v = x.var(dim=axis, keepdim=True, unbiased=False)
    return (x - m) / torch.sqrt(v + eps)
def _scatter_add(tgt, idx, src, axis=0):
    return tgt.clone().index_add_(axis, idx, src)
def _segment_sum(src, seg, n):
    out = torch.zeros((n,) + src.shape[1:], dtype=src.dtype)
    return out.index_add_(0, seg, src)
def _pad(x, pw):
    flat = tuple(v for pair in reversed(pw) for v in pair)
    return torch.nn.functional.pad(x, flat)

kernel_name = 'recurrent_scan__linear_attn_block__geglu_ffn__prompt'

def setup_inputs():

    _w = (lambda a: (lambda t: t.to(torch.bfloat16) if t.is_floating_point() else t)(torch.as_tensor(a)))
    def _A_setup():
        B, S, D = 1, 2048, 512
        return {
            "x": _w((lambda *s: torch.randn(*s, dtype=torch.bfloat16) * 0.05)(B, S, D)),
            "wqkv": _w((lambda *s: torch.randn(*s, dtype=torch.bfloat16) * 0.05)(D, 3 * D)),
            "wo": _w((lambda *s: torch.randn(*s, dtype=torch.bfloat16) * 0.05)(D, D)),
        }
    _A_in = _A_setup()

    def _B_setup():
        B, S, D, F = 1, 2048, 512, 1792
        return {
            "x": _w((lambda *s: torch.randn(*s, dtype=torch.bfloat16) * 0.05)(B, S, D)),
            "w_gate": _w((lambda *s: torch.randn(*s, dtype=torch.bfloat16) * 0.05)(D, F)),
            "w_up": _w((lambda *s: torch.randn(*s, dtype=torch.bfloat16) * 0.05)(D, F)),
            "w_down": _w((lambda *s: torch.randn(*s, dtype=torch.bfloat16) * 0.05)(F, D)),
        }
    _B_in = _B_setup()

    return {
        "A_x": _A_in["x"],
        "A_wqkv": _A_in["wqkv"],
        "A_wo": _A_in["wo"],
        "B_x": _B_in["x"],
        "B_w_gate": _B_in["w_gate"],
        "B_w_up": _B_in["w_up"],
        "B_w_down": _B_in["w_down"],
    }

def reference(A_x, A_wqkv, A_wo, B_x, B_w_gate, B_w_up, B_w_down):
    def _A_ref(x, wqkv, wo):
        B, S, D = x.shape
        H = 32; Dh = D // H
        qkv = (lambda x, *d: x.permute(*d))((x @ wqkv).reshape(B, S, 3, H, Dh), 2, 0, 3, 1, 4)
        q, k, v = qkv[0], qkv[1], qkv[2]
        phi = lambda t: torch.where(t > 0, t + 1, torch.exp(t)) + 1e-6
        pq, pk = phi(q), phi(k)
        kv = torch.einsum("bhsd,bhse->bhde", pk, v)
        z = torch.einsum("bhsd->bhd", pk)
        num = torch.einsum("bhqd,bhde->bhqe", pq, kv)
        den = torch.einsum("bhqd,bhd->bhq", pq, z)[..., None] + 1e-6
        o = num / den
        return (lambda x, *d: x.permute(*d))(o, 0, 2, 1, 3).reshape(B, S, D) @ wo

    def _B_ref(x, w_gate, w_up, w_down):
        return (_gelu(x @ w_gate) * (x @ w_up)) @ w_down

    x = A_x
    xn = _layernorm(x, axis=-1)
    s = _A_ref(xn, A_wqkv, A_wo)
    g = _silu(_B_ref(xn, B_w_gate, B_w_up, B_w_down))
    return x + s * g
```

```cpp
#include <hip/hip_runtime.h>
#include <hip/hip_bf16.h>
#include <stdint.h>

typedef __attribute__((ext_vector_type(8))) short s16x8;
typedef __attribute__((ext_vector_type(4))) short s16x4;
typedef __attribute__((ext_vector_type(16))) float f32x16;
typedef __attribute__((ext_vector_type(4))) float f32x4;
typedef __attribute__((ext_vector_type(4))) unsigned u32x4;
typedef __attribute__((ext_vector_type(2))) unsigned u32x2;
typedef unsigned short u16;
#define LAS __attribute__((address_space(3)))
#define DI __device__ __forceinline__

constexpr int S_ = 2048, D_ = 512, F_ = 1792, NH = 32;
constexpr int RT = S_ / 32;
constexpr int FT = F_ / 16;
constexpr size_t OFF_XNT = 0;
constexpr size_t OFF_HT  = OFF_XNT + (size_t)RT * 32 * 1024;
constexpr size_t OFF_PQ  = OFF_HT + (size_t)RT * FT * 1024;
constexpr size_t OFF_WDT = OFF_PQ + (size_t)RT * NH * 1024;
constexpr size_t OFF_WOT = OFF_WDT + (size_t)16 * FT * 1024;
constexpr size_t OFF_KV  = OFF_WOT + (size_t)16 * NH * 1024;
constexpr int KV_WORDS = NH * 272;

DI float bf2f(u16 v) { return __uint_as_float(((unsigned)v) << 16); }
DI u16 f2bf(float f) { __hip_bfloat16 b = __float2bfloat16(f); return *reinterpret_cast<u16*>(&b); }
DI float rbf(float f) { return bf2f(f2bf(f)); }
DI unsigned pack2(float lo, float hi) { return (unsigned)f2bf(lo) | ((unsigned)f2bf(hi) << 16); }
DI int kperm(int hh, int j) { return j < 4 ? 4 * hh + j : 8 + 4 * hh + (j - 4); }

DI float phi_bf(float t) {
  float a = t > 0.f ? rbf(t + 1.0f) : rbf(__expf(t));
  return rbf(a + 1e-6f);
}
DI float gelu_tanh(float x) {
  float u = 0.7978845608028654f * (x + 0.044715f * x * x * x);
  float e = __expf(2.0f * u);
  float th = 1.0f - 2.0f / (e + 1.0f);
  return 0.5f * x * (1.0f + th);
}
DI float wave_sum(float v) {
#pragma unroll
  for (int o = 1; o < 64; o <<= 1) v += __shfl_xor(v, o);
  return v;
}

__global__ __launch_bounds__(512) void k0_prep(const u16* __restrict__ x, const u16* __restrict__ wdown, const u16* __restrict__ wo,
                                               char* __restrict__ ws) {
  __shared__ __attribute__((aligned(16))) char stage[64 * 8 * 16];
  const int b = blockIdx.x, tid = threadIdx.x, l = tid & 63, w = tid >> 6;
  for (int t = w; t < 9; t += 8) {
    const int id = 9 * b + t;
    const int n32 = l & 31, hh = l >> 5;
    const u16* src; int krow0; char* dst;
    if (id < 16 * FT) { const int ct = id / FT, ft = id % FT; src = wdown + 32 * ct + n32; krow0 = 16 * ft; dst = ws + OFF_WDT + (size_t)id * 1024; }
    else { const int i2 = id - 16 * FT, ct = i2 / NH, hd = i2 % NH; src = wo + 32 * ct + n32; krow0 = 16 * hd; dst = ws + OFF_WOT + (size_t)i2 * 1024; }
    u16 v[8];
#pragma unroll
    for (int j = 0; j < 8; ++j) v[j] = src[(size_t)(krow0 + kperm(hh, j)) * D_];
    u32x4 o;
#pragma unroll
    for (int j = 0; j < 4; ++j) o[j] = (unsigned)v[2 * j] | ((unsigned)v[2 * j + 1] << 16);
    *reinterpret_cast<u32x4*>(dst + l * 16) = o;
  }
  { const int g = b * 512 + tid; if (g < KV_WORDS) reinterpret_cast<float*>(ws + OFF_KV)[g] = 0.f; }
  const int row = 8 * b + w;
  const u32x4 xv = *reinterpret_cast<const u32x4*>(x + (size_t)row * D_ + l * 8);
  float v[8];
#pragma unroll
  for (int j = 0; j < 4; ++j) { v[2 * j] = __uint_as_float(xv[j] << 16); v[2 * j + 1] = __uint_as_float(xv[j] & 0xffff0000u); }
  float sum = 0.f;
#pragma unroll
  for (int j = 0; j < 8; ++j) sum += v[j];
  const float mean = wave_sum(sum) * (1.0f / D_);
  float sq = 0.f;
#pragma unroll
  for (int j = 0; j < 8; ++j) { const float d = v[j] - mean; sq += d * d; }
  const float var = wave_sum(sq) * (1.0f / D_);
  const float mb = rbf(mean), vb = rbf(var);
  const float sd = rbf(__fsqrt_rn(rbf(vb + 1e-5f)));
  u32x4 o;
#pragma unroll
  for (int j = 0; j < 4; ++j) o[j] = pack2(__fdiv_rn(rbf(v[2 * j] - mb), sd), __fdiv_rn(rbf(v[2 * j + 1] - mb), sd));
  *reinterpret_cast<u32x4*>(stage + ((l >> 1) * 2 + (l & 1)) * 128 + w * 16) = o;
  __syncthreads();
  {
    const int seg = tid >> 3, p = tid & 7, ks = seg >> 1, hh = seg & 1;
    const int rt = row >> 5, s0 = (8 * b) & 31;
    *reinterpret_cast<u32x4*>(ws + OFF_XNT + ((size_t)(rt * 32 + ks) * 64 + hh * 32 + s0 + p) * 16) =
        *reinterpret_cast<const u32x4*>(stage + tid * 16);
  }
}

DI const u16* tile_src(int T, int plane, const u16* wqkv, const u16* wg, const u16* wu, int& ld) {
  if (T < FT) { ld = F_; return (plane ? wu : wg) + 16 * T; }
  ld = 3 * D_;
  if (T < FT + NH) return wqkv + D_ * (1 + plane) + 16 * (T - FT);
  return wqkv + 32 * (T - FT - NH) + 16 * plane;
}

constexpr int K1_LDS = 512 * 320;

#define XLOAD(dst, ptr) asm volatile("global_load_dwordx4 %0, %1, off" : "=v"(dst) : "v"(ptr) : "memory")

__global__ __launch_bounds__(512, 2) void k1_gemm(const u16* __restrict__ wqkv, const u16* __restrict__ wg, const u16* __restrict__ wu,
                                                  char* __restrict__ ws) {
  extern __shared__ __attribute__((aligned(16))) char lds[];
  const int tid = threadIdx.x, l = tid & 63, w = tid >> 6;
  const int bid = blockIdx.x, xcd = bid & 7, idx = bid >> 3;
  const int mb = 4 * (xcd & 1) + (idx & 3), nb = 8 * (xcd >> 1) + (idx >> 2);
  const int rt = 8 * mb + w;

  const u16* gsrc[5]; int gld[5];
#pragma unroll
  for (int bq = 0; bq < 5; ++bq) {
    const int q = 64 * bq + l, krow = q / 20, c = q % 20;
    const int plane = c / 10, i = (c % 10) >> 1, half = c & 1;
    int ld; const u16* p = tile_src(5 * nb + i, plane, wqkv, wg, wu, ld);
    gsrc[bq] = p + (size_t)(16 * w + krow) * ld + 8 * half; gld[bq] = ld;
  }
  const char* xbase = ws + OFF_XNT + (size_t)rt * 32 * 1024 + l * 16;

  s16x8 xf[32];
#pragma unroll
  for (int c = 0; c < 4; ++c) {
#pragma unroll
    for (int k = 0; k < 8; ++k) XLOAD(xf[8 * c + k], xbase + (8 * c + k) * 1024);
#pragma unroll
    for (int bq = 0; bq < 5; ++bq)
      __builtin_amdgcn_global_load_lds((const unsigned*)(gsrc[bq] + (size_t)c * 128 * gld[bq]),
                                       (unsigned*)(lds + (128 * c + 16 * w) * 320 + bq * 1024 + l * 16), 16, 0, 0);
  }

  f32x16 acc[5];
#pragma unroll
  for (int i = 0; i < 5; ++i)
#pragma unroll
    for (int r = 0; r < 16; ++r) acc[i][r] = 0.f;

  const int g4 = l >> 4, q4 = (l & 15) >> 2, p4 = l & 3;
  const int trbase = (8 * (g4 >> 1) + q4) * 320 + (g4 & 1) * 160 + p4 * 8;

#define K1_WAIT(N, C) asm volatile("s_waitcnt vmcnt(" #N ")" : "+v"(xf[8*C]), "+v"(xf[8*C+1]), "+v"(xf[8*C+2]), "+v"(xf[8*C+3]), \
      "+v"(xf[8*C+4]), "+v"(xf[8*C+5]), "+v"(xf[8*C+6]), "+v"(xf[8*C+7]) :: "memory")
#define K1_CHUNK(C) do { \
    __builtin_amdgcn_s_barrier(); asm volatile("" ::: "memory"); \
    _Pragma("unroll") for (int k = 0; k < 8; ++k) { \
      _Pragma("unroll") for (int i = 0; i < 5; ++i) { \
        const char* pa = lds + trbase + (128 * C + 16 * k) * 320 + i * 32; \
        s16x4 lo = __builtin_amdgcn_ds_read_tr16_b64_v4i16((LAS s16x4*)(pa)); \
        s16x4 hi = __builtin_amdgcn_ds_read_tr16_b64_v4i16((LAS s16x4*)(pa + 4 * 320)); \
        s16x8 wf = __builtin_shufflevector(lo, hi, 0, 1, 2, 3, 4, 5, 6, 7); \
        acc[i] = __builtin_amdgcn_mfma_f32_32x32x16_bf16(wf, xf[8 * C + k], acc[i], 0, 0, 0); \
      } } } while (0)

  K1_WAIT(39, 0); K1_CHUNK(0);
  K1_WAIT(26, 1); K1_CHUNK(1);
  K1_WAIT(13, 2); K1_CHUNK(2);
  K1_WAIT(0, 3);  K1_CHUNK(3);

  __syncthreads();
  float* kvred = reinterpret_cast<float*>(lds);
  char* ybuf = lds + 8192 + w * 2048;
  bool any_kv = false;
#pragma unroll
  for (int i = 0; i < 5; ++i) { const int T = 5 * nb + i; any_kv |= (T >= FT && T < FT + NH); }
  if (any_kv) { for (int e = tid; e < 5 * 272; e += 512) kvred[e] = 0.f; }
  __syncthreads();

  const int hh = l >> 5, s = l & 31;
#pragma unroll
  for (int i = 0; i < 5; ++i) {
    const int T = 5 * nb + i;
    if (T < FT) {
      u32x4 o;
#pragma unroll
      for (int j = 0; j < 4; ++j) {
        float h2[2];
#pragma unroll
        for (int e = 0; e < 2; ++e) {
          const float gt = rbf(acc[i][2 * j + e]), up = rbf(acc[i][8 + 2 * j + e]);
          h2[e] = rbf(gelu_tanh(gt)) * up;
        }
        o[j] = pack2(h2[0], h2[1]);
      }
      *reinterpret_cast<u32x4*>(ws + OFF_HT + ((size_t)(rt * FT + T) * 64 + l) * 16) = o;
    } else if (T >= FT + NH) {
      const int ha = 2 * (T - FT - NH);
      u32x4 oa, ob;
#pragma unroll
      for (int j = 0; j < 4; ++j) {
        oa[j] = pack2(phi_bf(rbf(acc[i][2 * j])), phi_bf(rbf(acc[i][2 * j + 1])));
        ob[j] = pack2(phi_bf(rbf(acc[i][8 + 2 * j])), phi_bf(rbf(acc[i][8 + 2 * j + 1])));
      }
      *reinterpret_cast<u32x4*>(ws + OFF_PQ + ((size_t)(rt * NH + ha) * 64 + l) * 16) = oa;
      *reinterpret_cast<u32x4*>(ws + OFF_PQ + ((size_t)(rt * NH + ha + 1) * 64 + l) * 16) = ob;
    } else {
#pragma unroll
      for (int gq = 0; gq < 4; ++gq) {
        u32x2 pk;
        float f0 = acc[i][4 * gq], f1 = acc[i][4 * gq + 1], f2 = acc[i][4 * gq + 2], f3 = acc[i][4 * gq + 3];
        if (gq < 2) { pk[0] = pack2(phi_bf(rbf(f0)), phi_bf(rbf(f1))); pk[1] = pack2(phi_bf(rbf(f2)), phi_bf(rbf(f3))); }
        else        { pk[0] = pack2(f0, f1); pk[1] = pack2(f2, f3); }
        *reinterpret_cast<u32x2*>(ybuf + s * 64 + (8 * gq + 4 * hh) * 2) = pk;
      }
      asm volatile("s_waitcnt lgkmcnt(0)" ::: "memory");
      f32x16 dd, dz;
#pragma unroll
      for (int r = 0; r < 16; ++r) { dd[r] = 0.f; dz[r] = 0.f; }
      s16x8 ones;
#pragma unroll
      for (int j = 0; j < 8; ++j) ones[j] = (short)0x3f80;
#pragma unroll
      for (int st = 0; st < 2; ++st) {
        const char* pa = ybuf + (16 * st + 8 * (g4 >> 1) + q4) * 64 + (g4 & 1) * 32 + p4 * 8;
        s16x4 lo = __builtin_amdgcn_ds_read_tr16_b64_v4i16((LAS s16x4*)(pa));
        s16x4 hi = __builtin_amdgcn_ds_read_tr16_b64_v4i16((LAS s16x4*)(pa + 4 * 64));
        s16x8 yf = __builtin_shufflevector(lo, hi, 0, 1, 2, 3, 4, 5, 6, 7);
        dd = __builtin_amdgcn_mfma_f32_32x32x16_bf16(yf, yf, dd, 0, 0, 0);
        dz = __builtin_amdgcn_mfma_f32_32x32x16_bf16(yf, ones, dz, 0, 0, 0);
      }
      float* kr = kvred + i * 272;
      if (s >= 16) {
#pragma unroll
        for (int r = 0; r < 8; ++r) atomicAdd(kr + ((r & 3) + 8 * (r >> 2) + 4 * hh) * 16 + (s - 16), dd[r]);
      } else if (s == 0) {
#pragma unroll
        for (int r = 0; r < 8; ++r) atomicAdd(kr + 256 + (r & 3) + 8 * (r >> 2) + 4 * hh, dz[r]);
      }
      asm volatile("s_waitcnt lgkmcnt(0)" ::: "memory");
    }
  }
  if (any_kv) {
    __syncthreads();
    for (int e = tid; e < 5 * 272; e += 512) {
      const int i = e / 272, T = 5 * nb + i;
      if (T >= FT && T < FT + NH) atomicAdd(reinterpret_cast<float*>(ws + OFF_KV) + (T - FT) * 272 + (e - i * 272), kvred[e]);
    }
  }
}

constexpr int K2_LDS = 4 * 32768;

__global__ __launch_bounds__(256, 1) void k2_gemm(const u16* __restrict__ x, const char* __restrict__ ws, u16* __restrict__ out) {
  extern __shared__ __attribute__((aligned(16))) char lds[];
  const int tid = threadIdx.x, l = tid & 63, w = tid >> 6;
  const int bid = blockIdx.x, xcd = bid & 7, idx = bid >> 3;
  const int rb = 4 * xcd + (idx >> 3), cb = idx & 7;
  const int rt0 = 2 * rb, ct0 = 2 * cb;

  {
    const float* kvacc = reinterpret_cast<const float*>(ws + OFF_KV);
    for (int it = tid; it < NH * 64; it += 256) {
      const int hd = it >> 6, L = it & 63, r32 = L & 31, h2 = L >> 5;
      u32x4 o;
#pragma unroll
      for (int j = 0; j < 4; ++j) {
        const int d0 = kperm(h2, 2 * j), d1 = kperm(h2, 2 * j + 1);
        const float a = r32 < 16 ? kvacc[hd * 272 + d0 * 16 + r32] : kvacc[hd * 272 + 256 + d0];
        const float c = r32 < 16 ? kvacc[hd * 272 + d1 * 16 + r32] : kvacc[hd * 272 + 256 + d1];
        o[j] = pack2(a, c);
      }
      *reinterpret_cast<u32x4*>(lds + it * 16) = o;
    }
  }
  __syncthreads();

  f32x16 ag[2][2], as[2][2];
#pragma unroll
  for (int c = 0; c < 2; ++c)
#pragma unroll
    for (int r = 0; r < 2; ++r)
#pragma unroll
      for (int e = 0; e < 16; ++e) { ag[c][r][e] = 0.f; as[c][r][e] = 0.f; }

  {
    const char* hb0 = ws + OFF_HT + ((size_t)(rt0 * FT) * 64 + l) * 16;
    const char* hb1 = hb0 + (size_t)FT * 1024;
    const char* wa0 = ws + OFF_WDT + ((size_t)(ct0 * FT) * 64 + l) * 16;
    const char* wa1 = wa0 + (size_t)FT * 1024;
    const int f0 = 28 * w;
#pragma unroll 4
    for (int f = 0; f < 28; ++f) {
      const size_t o = (size_t)(f0 + f) * 1024;
      const s16x8 b0 = *reinterpret_cast<const s16x8*>(hb0 + o), b1 = *reinterpret_cast<const s16x8*>(hb1 + o);
      const s16x8 a0 = *reinterpret_cast<const s16x8*>(wa0 + o), a1 = *reinterpret_cast<const s16x8*>(wa1 + o);
      ag[0][0] = __builtin_amdgcn_mfma_f32_32x32x16_bf16(a0, b0, ag[0][0], 0, 0, 0);
      ag[0][1] = __builtin_amdgcn_mfma_f32_32x32x16_bf16(a0, b1, ag[0][1], 0, 0, 0);
      ag[1][0] = __builtin_amdgcn_mfma_f32_32x32x16_bf16(a1, b0, ag[1][0], 0, 0, 0);
      ag[1][1] = __builtin_amdgcn_mfma_f32_32x32x16_bf16(a1, b1, ag[1][1], 0, 0, 0);
    }
  }
  {
#pragma unroll 2
    for (int hq = 0; hq < 8; ++hq) {
      const int hd = 8 * w + hq;
      const s16x8 kf = *reinterpret_cast<const s16x8*>(lds + (hd * 64 + l) * 16);
      const s16x8 a0 = *reinterpret_cast<const s16x8*>(ws + OFF_WOT + ((size_t)(ct0 * NH + hd) * 64 + l) * 16);
      const s16x8 a1 = *reinterpret_cast<const s16x8*>(ws + OFF_WOT + ((size_t)((ct0 + 1) * NH + hd) * 64 + l) * 16);
#pragma unroll
      for (int r = 0; r < 2; ++r) {
        const s16x8 pq = *reinterpret_cast<const s16x8*>(ws + OFF_PQ + ((size_t)((rt0 + r) * NH + hd) * 64 + l) * 16);
        f32x16 nd;
#pragma unroll
        for (int e = 0; e < 16; ++e) nd[e] = 0.f;
        nd = __builtin_amdgcn_mfma_f32_32x32x16_bf16(kf, pq, nd, 0, 0, 0);
        const float den = rbf(rbf(nd[8]) + 1e-6f);
        u32x4 ob;
#pragma unroll
        for (int j = 0; j < 4; ++j) ob[j] = pack2(__fdiv_rn(rbf(nd[2 * j]), den), __fdiv_rn(rbf(nd[2 * j + 1]), den));
        const s16x8 of = __builtin_bit_cast(s16x8, ob);
        as[0][r] = __builtin_amdgcn_mfma_f32_32x32x16_bf16(a0, of, as[0][r], 0, 0, 0);
        as[1][r] = __builtin_amdgcn_mfma_f32_32x32x16_bf16(a1, of, as[1][r], 0, 0, 0);
      }
    }
  }
  __syncthreads();
#pragma unroll
  for (int c = 0; c < 2; ++c)
#pragma unroll
    for (int r = 0; r < 2; ++r)
#pragma unroll
      for (int qd = 0; qd < 4; ++qd) {
        f32x4 vg = {ag[c][r][4 * qd], ag[c][r][4 * qd + 1], ag[c][r][4 * qd + 2], ag[c][r][4 * qd + 3]};
        f32x4 vs = {as[c][r][4 * qd], as[c][r][4 * qd + 1], as[c][r][4 * qd + 2], as[c][r][4 * qd + 3]};
        *reinterpret_cast<f32x4*>(lds + ((((w * 8 + c * 2 + r) * 4 + qd) * 64 + l) * 16)) = vg;
        *reinterpret_cast<f32x4*>(lds + ((((w * 8 + 4 + c * 2 + r) * 4 + qd) * 64 + l) * 16)) = vs;
      }
  __syncthreads();
#pragma unroll
  for (int it = 0; it < 4; ++it) {
    const int Q = it * 256 + tid, row = Q >> 4, nq = Q & 15;
    const int r = row >> 5, s = row & 31, c = nq >> 3, hh = nq & 1, qd = (nq & 7) >> 1;
    f32x4 vg = {0.f, 0.f, 0.f, 0.f}, vs = {0.f, 0.f, 0.f, 0.f};
#pragma unroll
    for (int ww = 0; ww < 4; ++ww) {
      vg += *reinterpret_cast<const f32x4*>(lds + ((((ww * 8 + c * 2 + r) * 4 + qd) * 64 + hh * 32 + s) * 16));
      vs += *reinterpret_cast<const f32x4*>(lds + ((((ww * 8 + 4 + c * 2 + r) * 4 + qd) * 64 + hh * 32 + s) * 16));
    }
    const size_t go = (size_t)(64 * rb + row) * D_ + 64 * cb + 32 * c + 8 * qd + 4 * hh;
    const u32x2 xv = *reinterpret_cast<const u32x2*>(x + go);
    const float xs[4] = {__uint_as_float(xv[0] << 16), __uint_as_float(xv[0] & 0xffff0000u), __uint_as_float(xv[1] << 16), __uint_as_float(xv[1] & 0xffff0000u)};
    float res[4];
#pragma unroll
    for (int e = 0; e < 4; ++e) {
      const float gp = rbf(vg[e]);
      const float gs = rbf(gp / (1.0f + __expf(-gp)));
      res[e] = xs[e] + rbf(rbf(vs[e]) * gs);
    }
    u32x2 ov = {pack2(res[0], res[1]), pack2(res[2], res[3])};
    *reinterpret_cast<u32x2*>(out + go) = ov;
  }
}

extern "C" void kernel_launch(void* const* d_in, const int* in_sizes, int n_in,
                              void* d_out, int out_size, void* d_ws, size_t ws_size,
                              hipStream_t stream) {
  const u16* x = (const u16*)d_in[0];
  const u16* wqkv = (const u16*)d_in[1];
  const u16* wo = (const u16*)d_in[2];
  const u16* wg = (const u16*)d_in[4];
  const u16* wu = (const u16*)d_in[5];
  const u16* wd = (const u16*)d_in[6];
  char* ws = (char*)d_ws;
  (void)hipFuncSetAttribute((const void*)k1_gemm, hipFuncAttributeMaxDynamicSharedMemorySize, K1_LDS);
  (void)hipFuncSetAttribute((const void*)k2_gemm, hipFuncAttributeMaxDynamicSharedMemorySize, K2_LDS);
  k0_prep<<<256, 512, 0, stream>>>(x, wd, wo, ws);
  k1_gemm<<<256, 512, K1_LDS, stream>>>(wqkv, wg, wu, ws);
  k2_gemm<<<256, 256, K2_LDS, stream>>>(x, ws, (u16*)d_out);
}
```

```cpp
#include <hip/hip_runtime.h>
#include <hip/hip_bf16.h>
#include <stdint.h>

typedef __attribute__((ext_vector_type(8))) short s16x8;
typedef __attribute__((ext_vector_type(4))) short s16x4;
typedef __attribute__((ext_vector_type(16))) float f32x16;
typedef __attribute__((ext_vector_type(4))) float f32x4;
typedef __attribute__((ext_vector_type(4))) unsigned u32x4;
typedef __attribute__((ext_vector_type(2))) unsigned u32x2;
typedef unsigned short u16;
#define LAS __attribute__((address_space(3)))
#define DI __device__ __forceinline__

constexpr int S_ = 2048, D_ = 512, F_ = 1792, NH = 32;
constexpr int RT = S_ / 32;
constexpr int FT = F_ / 16;
constexpr size_t OFF_XNT = 0;
constexpr size_t OFF_HT  = OFF_XNT + (size_t)RT * 32 * 1024;
constexpr size_t OFF_PQ  = OFF_HT + (size_t)RT * FT * 1024;
constexpr size_t OFF_WDT = OFF_PQ + (size_t)RT * NH * 1024;
constexpr size_t OFF_WOT = OFF_WDT + (size_t)16 * FT * 1024;
constexpr size_t OFF_KV  = OFF_WOT + (size_t)16 * NH * 1024;
constexpr int KV_WORDS = NH * 272;

DI float bf2f(u16 v) { return __uint_as_float(((unsigned)v) << 16); }
DI u16 f2bf(float f) { __hip_bfloat16 b = __float2bfloat16(f); return *reinterpret_cast<u16*>(&b); }
DI float rbf(float f) { return bf2f(f2bf(f)); }
typedef __attribute__((ext_vector_type(2))) __bf16 bf16x2;
typedef __attribute__((ext_vector_type(2))) float f32x2;
DI unsigned pack2(float lo, float hi) {
  f32x2 v = {lo, hi};
  return __builtin_bit_cast(unsigned, __builtin_convertvector(v, bf16x2));
}
DI float unlo(unsigned p) { return __uint_as_float(p << 16); }
DI float unhi(unsigned p) { return __uint_as_float(p & 0xffff0000u); }
DI int kperm(int hh, int j) { return j < 4 ? 4 * hh + j : 8 + 4 * hh + (j - 4); }

DI float phi_bf(float t) {
  float a = t > 0.f ? rbf(t + 1.0f) : rbf(__expf(t));
  return rbf(a + 1e-6f);
}
DI float gelu_tanh(float x) {
  const float y = x * __builtin_fmaf(x * x, 0.10294323970f, 2.30220819811f);
  const float r = __builtin_amdgcn_rcpf(1.0f + __builtin_amdgcn_exp2f(y));
  return __builtin_fmaf(-x, r, x);
}
DI float wave_sum(float v) {
#pragma unroll
  for (int o = 1; o < 64; o <<= 1) v += __shfl_xor(v, o);
  return v;
}

__global__ __launch_bounds__(512) void k0_prep(const u16* __restrict__ x, const u16* __restrict__ wdown, const u16* __restrict__ wo,
                                               char* __restrict__ ws) {
  __shared__ __attribute__((aligned(16))) char stage[64 * 8 * 16];
  const int b = blockIdx.x, tid = threadIdx.x, l = tid & 63, w = tid >> 6;
  for (int t = w; t < 9; t += 8) {
    const int id = 9 * b + t;
    const int n32 = l & 31, hh = l >> 5;
    const u16* src; int krow0; char* dst;
    if (id < 16 * FT) { const int ct = id / FT, ft = id % FT; src = wdown + 32 * ct + n32; krow0 = 16 * ft; dst = ws + OFF_WDT + (size_t)id * 1024; }
    else { const int i2 = id - 16 * FT, ct = i2 / NH, hd = i2 % NH; src = wo + 32 * ct + n32; krow0 = 16 * hd; dst = ws + OFF_WOT + (size_t)i2 * 1024; }
    u16 v[8];
#pragma unroll
    for (int j = 0; j < 8; ++j) v[j] = src[(size_t)(krow0 + kperm(hh, j)) * D_];
    u32x4 o;
#pragma unroll
    for (int j = 0; j < 4; ++j) o[j] = (unsigned)v[2 * j] | ((unsigned)v[2 * j + 1] << 16);
    *reinterpret_cast<u32x4*>(dst + l * 16) = o;
  }
  { const int g = b * 512 + tid; if (g < KV_WORDS) reinterpret_cast<float*>(ws + OFF_KV)[g] = 0.f; }
  const int row = 8 * b + w;
  const u32x4 xv = *reinterpret_cast<const u32x4*>(x + (size_t)row * D_ + l * 8);
  float v[8];
#pragma unroll
  for (int j = 0; j < 4; ++j) { v[2 * j] = __uint_as_float(xv[j] << 16); v[2 * j + 1] = __uint_as_float(xv[j] & 0xffff0000u); }
  float sum = 0.f;
#pragma unroll
  for (int j = 0; j < 8; ++j) sum += v[j];
  const float mean = wave_sum(sum) * (1.0f / D_);
  float sq = 0.f;
#pragma unroll
  for (int j = 0; j < 8; ++j) { const float d = v[j] - mean; sq += d * d; }
  const float var = wave_sum(sq) * (1.0f / D_);
  const float mb = rbf(mean), vb = rbf(var);
  const float sd = rbf(__fsqrt_rn(rbf(vb + 1e-5f)));
  u32x4 o;
#pragma unroll
  for (int j = 0; j < 4; ++j) o[j] = pack2(__fdiv_rn(rbf(v[2 * j] - mb), sd), __fdiv_rn(rbf(v[2 * j + 1] - mb), sd));
  *reinterpret_cast<u32x4*>(stage + ((l >> 1) * 2 + (l & 1)) * 128 + w * 16) = o;
  __syncthreads();
  {
    const int seg = tid >> 3, p = tid & 7, ks = seg >> 1, hh = seg & 1;
    const int rt = row >> 5, s0 = (8 * b) & 31;
    *reinterpret_cast<u32x4*>(ws + OFF_XNT + ((size_t)(rt * 32 + ks) * 64 + hh * 32 + s0 + p) * 16) =
        *reinterpret_cast<const u32x4*>(stage + tid * 16);
  }
}

DI const u16* tile_src(int T, int plane, const u16* wqkv, const u16* wg, const u16* wu, int& ld) {
  if (T < FT) { ld = F_; return (plane ? wu : wg) + 16 * T; }
  ld = 3 * D_;
  if (T < FT + NH) return wqkv + D_ * (1 + plane) + 16 * (T - FT);
  return wqkv + 32 * (T - FT - NH) + 16 * plane;
}

constexpr int K1_LDS = 512 * 320;

#define XLOAD(dst, ptr) asm volatile("global_load_dwordx4 %0, %1, off" : "=v"(dst) : "v"(ptr) : "memory")
DI void glds16(const void* gsrc, unsigned lds_dst) {
  unsigned keep;
  asm volatile("s_mov_b32 %0, m0\n\ts_mov_b32 m0, %2\n\ts_nop 0\n\tglobal_load_lds_dwordx4 %1, off\n\ts_mov_b32 m0, %0"
               : "=&s"(keep) : "v"(gsrc), "s"(lds_dst) : "memory");
}

__global__ __launch_bounds__(512, 2) void k1_gemm(const u16* __restrict__ wqkv, const u16* __restrict__ wg, const u16* __restrict__ wu,
                                                  char* __restrict__ ws) {
  extern __shared__ __attribute__((aligned(16))) char lds[];
  const int tid = threadIdx.x, l = tid & 63, w = tid >> 6;
  const int bid = blockIdx.x, xcd = bid & 7, idx = bid >> 3;
  const int mb = 4 * (xcd & 1) + (idx & 3), nb = 8 * (xcd >> 1) + (idx >> 2);
  const int rt = 8 * mb + w;

  const u16* gsrc[5]; int gld[5];
#pragma unroll
  for (int bq = 0; bq < 5; ++bq) {
    const int q = 64 * bq + l, krow = q / 20, c = q % 20;
    const int plane = c / 10, i = (c % 10) >> 1, half = c & 1;
    int ld; const u16* p = tile_src(5 * nb + i, plane, wqkv, wg, wu, ld);
    gsrc[bq] = p + (size_t)(16 * w + krow) * ld + 8 * half; gld[bq] = ld;
  }
  const char* xbase = ws + OFF_XNT + (size_t)rt * 32 * 1024 + l * 16;

  s16x8 xf[32];
  const unsigned ldsw = __builtin_amdgcn_readfirstlane((unsigned)(size_t)lds + w * 16 * 320);
#define K1_ISSUE(C) do { \
    _Pragma("unroll") for (int k = 0; k < 8; ++k) XLOAD(xf[8 * (C) + k], xbase + (8 * (C) + k) * 1024); \
    _Pragma("unroll") for (int bq = 0; bq < 5; ++bq) glds16(gsrc[bq] + (size_t)(C) * 128 * gld[bq], ldsw + (C) * 128 * 320 + bq * 1024); \
  } while (0)
  K1_ISSUE(0); K1_ISSUE(1);

  f32x16 acc[5];
#pragma unroll
  for (int i = 0; i < 5; ++i)
#pragma unroll
    for (int r = 0; r < 16; ++r) acc[i][r] = 0.f;

  const int g4 = l >> 4, q4 = (l & 15) >> 2, p4 = l & 3;
  const int trbase = (8 * (g4 >> 1) + q4) * 320 + (g4 & 1) * 160 + p4 * 8;

#define K1_WAIT(N, C) asm volatile("s_waitcnt vmcnt(" #N ")" : "+v"(xf[8*C]), "+v"(xf[8*C+1]), "+v"(xf[8*C+2]), "+v"(xf[8*C+3]), \
      "+v"(xf[8*C+4]), "+v"(xf[8*C+5]), "+v"(xf[8*C+6]), "+v"(xf[8*C+7]) :: "memory")
#define K1_CHUNK(C, NC) do { \
    __builtin_amdgcn_s_barrier(); asm volatile("" ::: "memory"); \
    _Pragma("unroll") for (int k = 0; k < 8; ++k) { \
      if (NC < 4) { XLOAD(xf[(8 * NC + k) & 31], xbase + (8 * NC + k) * 1024); \
        if (k < 5) glds16(gsrc[k] + (size_t)(NC) * 128 * gld[k], ldsw + (NC) * 128 * 320 + k * 1024); } \
      _Pragma("unroll") for (int i = 0; i < 5; ++i) { \
        const char* pa = lds + trbase + (128 * C + 16 * k) * 320 + i * 32; \
        s16x4 lo = __builtin_amdgcn_ds_read_tr16_b64_v4i16((LAS s16x4*)(pa)); \
        s16x4 hi = __builtin_amdgcn_ds_read_tr16_b64_v4i16((LAS s16x4*)(pa + 4 * 320)); \
        s16x8 wf = __builtin_shufflevector(lo, hi, 0, 1, 2, 3, 4, 5, 6, 7); \
        acc[i] = __builtin_amdgcn_mfma_f32_32x32x16_bf16(wf, xf[8 * C + k], acc[i], 0, 0, 0); \
      } } } while (0)

  K1_WAIT(13, 0); K1_CHUNK(0, 2);
  K1_WAIT(13, 1); K1_CHUNK(1, 3);
  K1_WAIT(13, 2); K1_CHUNK(2, 4);
  K1_WAIT(0, 3);  K1_CHUNK(3, 4);

  __syncthreads();
  float* kvred = reinterpret_cast<float*>(lds);
  char* ybuf = lds + 8 * 5 * 272 * 4 + w * 2048;
  bool any_kv = false;
#pragma unroll
  for (int i = 0; i < 5; ++i) { const int T = 5 * nb + i; any_kv |= (T >= FT && T < FT + NH); }

  const int hh = l >> 5, s = l & 31;
#pragma unroll
  for (int i = 0; i < 5; ++i) {
    const int T = 5 * nb + i;
    if (T < FT) {
      u32x4 o;
#pragma unroll
      for (int j = 0; j < 4; ++j) {
        const unsigned gp = pack2(acc[i][2 * j], acc[i][2 * j + 1]), up = pack2(acc[i][8 + 2 * j], acc[i][8 + 2 * j + 1]);
        const unsigned lp = pack2(gelu_tanh(unlo(gp)), gelu_tanh(unhi(gp)));
        o[j] = pack2(unlo(lp) * unlo(up), unhi(lp) * unhi(up));
      }
      *reinterpret_cast<u32x4*>(ws + OFF_HT + ((size_t)(rt * FT + T) * 64 + l) * 16) = o;
    } else if (T >= FT + NH) {
      const int ha = 2 * (T - FT - NH);
      u32x4 oa, ob;
#pragma unroll
      for (int j = 0; j < 4; ++j) {
        oa[j] = pack2(phi_bf(rbf(acc[i][2 * j])), phi_bf(rbf(acc[i][2 * j + 1])));
        ob[j] = pack2(phi_bf(rbf(acc[i][8 + 2 * j])), phi_bf(rbf(acc[i][8 + 2 * j + 1])));
      }
      *reinterpret_cast<u32x4*>(ws + OFF_PQ + ((size_t)(rt * NH + ha) * 64 + l) * 16) = oa;
      *reinterpret_cast<u32x4*>(ws + OFF_PQ + ((size_t)(rt * NH + ha + 1) * 64 + l) * 16) = ob;
    } else {
#pragma unroll
      for (int gq = 0; gq < 4; ++gq) {
        u32x2 pk;
        float f0 = acc[i][4 * gq], f1 = acc[i][4 * gq + 1], f2 = acc[i][4 * gq + 2], f3 = acc[i][4 * gq + 3];
        if (gq < 2) { pk[0] = pack2(phi_bf(rbf(f0)), phi_bf(rbf(f1))); pk[1] = pack2(phi_bf(rbf(f2)), phi_bf(rbf(f3))); }
        else        { pk[0] = pack2(f0, f1); pk[1] = pack2(f2, f3); }
        *reinterpret_cast<u32x2*>(ybuf + s * 64 + (8 * gq + 4 * hh) * 2) = pk;
      }
      asm volatile("s_waitcnt lgkmcnt(0)" ::: "memory");
      f32x16 dd, dz;
#pragma unroll
      for (int r = 0; r < 16; ++r) { dd[r] = 0.f; dz[r] = 0.f; }
      s16x8 ones;
#pragma unroll
      for (int j = 0; j < 8; ++j) ones[j] = (short)0x3f80;
#pragma unroll
      for (int st = 0; st < 2; ++st) {
        const char* pa = ybuf + (16 * st + 8 * (g4 >> 1) + q4) * 64 + (g4 & 1) * 32 + p4 * 8;
        s16x4 lo = __builtin_amdgcn_ds_read_tr16_b64_v4i16((LAS s16x4*)(pa));
        s16x4 hi = __builtin_amdgcn_ds_read_tr16_b64_v4i16((LAS s16x4*)(pa + 4 * 64));
        s16x8 yf = __builtin_shufflevector(lo, hi, 0, 1, 2, 3, 4, 5, 6, 7);
        dd = __builtin_amdgcn_mfma_f32_32x32x16_bf16(yf, yf, dd, 0, 0, 0);
        dz = __builtin_amdgcn_mfma_f32_32x32x16_bf16(yf, ones, dz, 0, 0, 0);
      }
      float* kr = kvred + (w * 5 + i) * 272;
      if (s >= 16) {
#pragma unroll
        for (int r = 0; r < 8; ++r) kr[((r & 3) + 8 * (r >> 2) + 4 * hh) * 16 + (s - 16)] = dd[r];
      } else if (s == 0) {
#pragma unroll
        for (int r = 0; r < 8; ++r) kr[256 + (r & 3) + 8 * (r >> 2) + 4 * hh] = dz[r];
      }
      asm volatile("s_waitcnt lgkmcnt(0)" ::: "memory");
    }
  }
  if (any_kv) {
    __syncthreads();
    for (int e = tid; e < 5 * 272; e += 512) {
      const int i = e / 272, T = 5 * nb + i;
      if (T >= FT && T < FT + NH) {
        float a = 0.f;
#pragma unroll
        for (int ww = 0; ww < 8; ++ww) a += kvred[ww * 5 * 272 + e];
        atomicAdd(reinterpret_cast<float*>(ws + OFF_KV) + (T - FT) * 272 + (e - i * 272), a);
      }
    }
  }
}

constexpr int K2_LDS = 4 * 32768;

__global__ __launch_bounds__(256, 1) void k2_gemm(const u16* __restrict__ x, const char* __restrict__ ws, u16* __restrict__ out) {
  extern __shared__ __attribute__((aligned(16))) char lds[];
  const int tid = threadIdx.x, l = tid & 63, w = tid >> 6;
  const int bid = blockIdx.x, xcd = bid & 7, idx = bid >> 3;
  const int rb = 4 * xcd + (idx >> 3), cb = idx & 7;
  const int rt0 = 2 * rb, ct0 = 2 * cb;

  {
    const float* kvacc = reinterpret_cast<const float*>(ws + OFF_KV);
    for (int it = tid; it < NH * 64; it += 256) {
      const int hd = it >> 6, L = it & 63, r32 = L & 31, h2 = L >> 5;
      u32x4 o;
#pragma unroll
      for (int j = 0; j < 4; ++j) {
        const int d0 = kperm(h2, 2 * j), d1 = kperm(h2, 2 * j + 1);
        const float a = r32 < 16 ? kvacc[hd * 272 + d0 * 16 + r32] : kvacc[hd * 272 + 256 + d0];
        const float c = r32 < 16 ? kvacc[hd * 272 + d1 * 16 + r32] : kvacc[hd * 272 + 256 + d1];
        o[j] = pack2(a, c);
      }
      *reinterpret_cast<u32x4*>(lds + it * 16) = o;
    }
  }
  __syncthreads();

  f32x16 ag[2][2], as[2][2];
#pragma unroll
  for (int c = 0; c < 2; ++c)
#pragma unroll
    for (int r = 0; r < 2; ++r)
#pragma unroll
      for (int e = 0; e < 16; ++e) { ag[c][r][e] = 0.f; as[c][r][e] = 0.f; }

  {
    constexpr int NST = 36, R = 7;
    const char* hb0 = ws + OFF_HT + ((size_t)(rt0 * FT + 28 * w) * 64 + l) * 16;
    const char* wa0 = ws + OFF_WDT + ((size_t)(ct0 * FT + 28 * w) * 64 + l) * 16;
    const char* pq0 = ws + OFF_PQ + ((size_t)(rt0 * NH + 8 * w) * 64 + l) * 16;
    const char* wo0 = ws + OFF_WOT + ((size_t)(ct0 * NH + 8 * w) * 64 + l) * 16;
    s16x8 ring[R][4];
#define K2_ISSUE(ST) do { \
      if ((ST) < 28) { XLOAD(ring[(ST) % R][0], hb0 + (ST) * 1024); XLOAD(ring[(ST) % R][1], hb0 + (size_t)FT * 1024 + (ST) * 1024); \
                       XLOAD(ring[(ST) % R][2], wa0 + (ST) * 1024); XLOAD(ring[(ST) % R][3], wa0 + (size_t)FT * 1024 + (ST) * 1024); } \
      else           { XLOAD(ring[(ST) % R][0], pq0 + ((ST) - 28) * 1024); XLOAD(ring[(ST) % R][1], pq0 + (size_t)NH * 1024 + ((ST) - 28) * 1024); \
                       XLOAD(ring[(ST) % R][2], wo0 + ((ST) - 28) * 1024); XLOAD(ring[(ST) % R][3], wo0 + (size_t)NH * 1024 + ((ST) - 28) * 1024); } \
    } while (0)
#pragma unroll
    for (int st = 0; st < R - 1; ++st) K2_ISSUE(st);
#pragma unroll
    for (int cs = 0; cs < NST; ++cs) {
      if (cs + R - 1 < NST) K2_ISSUE(cs + R - 1);
      constexpr int dummy = 0; (void)dummy;
      const int ahead = (NST - 1 - cs) < (R - 1) ? (NST - 1 - cs) : (R - 1);
      s16x8* f = ring[cs % R];
      asm volatile("s_waitcnt vmcnt(%4)" : "+v"(f[0]), "+v"(f[1]), "+v"(f[2]), "+v"(f[3]) : "i"(4 * ahead) : "memory");
      if (cs < 28) {
        ag[0][0] = __builtin_amdgcn_mfma_f32_32x32x16_bf16(f[2], f[0], ag[0][0], 0, 0, 0);
        ag[0][1] = __builtin_amdgcn_mfma_f32_32x32x16_bf16(f[2], f[1], ag[0][1], 0, 0, 0);
        ag[1][0] = __builtin_amdgcn_mfma_f32_32x32x16_bf16(f[3], f[0], ag[1][0], 0, 0, 0);
        ag[1][1] = __builtin_amdgcn_mfma_f32_32x32x16_bf16(f[3], f[1], ag[1][1], 0, 0, 0);
      } else {
        const int hd = 8 * w + (cs - 28);
        const s16x8 kf = *reinterpret_cast<const s16x8*>(lds + (hd * 64 + l) * 16);
#pragma unroll
        for (int r = 0; r < 2; ++r) {
          f32x16 nd;
#pragma unroll
          for (int e = 0; e < 16; ++e) nd[e] = 0.f;
          nd = __builtin_amdgcn_mfma_f32_32x32x16_bf16(kf, f[r], nd, 0, 0, 0);
          const float rden = __builtin_amdgcn_rcpf(rbf(rbf(nd[8]) + 1e-6f));
          u32x4 ob;
#pragma unroll
          for (int j = 0; j < 4; ++j) {
            const unsigned np = pack2(nd[2 * j], nd[2 * j + 1]);
            ob[j] = pack2(unlo(np) * rden, unhi(np) * rden);
          }
          const s16x8 of = __builtin_bit_cast(s16x8, ob);
          as[0][r] = __builtin_amdgcn_mfma_f32_32x32x16_bf16(f[2], of, as[0][r], 0, 0, 0);
          as[1][r] = __builtin_amdgcn_mfma_f32_32x32x16_bf16(f[3], of, as[1][r], 0, 0, 0);
        }
      }
    }
  }
  __syncthreads();
#pragma unroll
  for (int c = 0; c < 2; ++c)
#pragma unroll
    for (int r = 0; r < 2; ++r)
#pragma unroll
      for (int qd = 0; qd < 4; ++qd) {
        f32x4 vg = {ag[c][r][4 * qd], ag[c][r][4 * qd + 1], ag[c][r][4 * qd + 2], ag[c][r][4 * qd + 3]};
        f32x4 vs = {as[c][r][4 * qd], as[c][r][4 * qd + 1], as[c][r][4 * qd + 2], as[c][r][4 * qd + 3]};
        *reinterpret_cast<f32x4*>(lds + ((((w * 8 + c * 2 + r) * 4 + qd) * 64 + l) * 16)) = vg;
        *reinterpret_cast<f32x4*>(lds + ((((w * 8 + 4 + c * 2 + r) * 4 + qd) * 64 + l) * 16)) = vs;
      }
  __syncthreads();
#pragma unroll
  for (int it = 0; it < 4; ++it) {
    const int Q = it * 256 + tid, row = Q >> 4, nq = Q & 15;
    const int r = row >> 5, s = row & 31, c = nq >> 3, hh = nq & 1, qd = (nq & 7) >> 1;
    f32x4 vg = {0.f, 0.f, 0.f, 0.f}, vs = {0.f, 0.f, 0.f, 0.f};
#pragma unroll
    for (int ww = 0; ww < 4; ++ww) {
      vg += *reinterpret_cast<const f32x4*>(lds + ((((ww * 8 + c * 2 + r) * 4 + qd) * 64 + hh * 32 + s) * 16));
      vs += *reinterpret_cast<const f32x4*>(lds + ((((ww * 8 + 4 + c * 2 + r) * 4 + qd) * 64 + hh * 32 + s) * 16));
    }
    const size_t go = (size_t)(64 * rb + row) * D_ + 64 * cb + 32 * c + 8 * qd + 4 * hh;
    const u32x2 xv = *reinterpret_cast<const u32x2*>(x + go);
    const float xs[4] = {__uint_as_float(xv[0] << 16), __uint_as_float(xv[0] & 0xffff0000u), __uint_as_float(xv[1] << 16), __uint_as_float(xv[1] & 0xffff0000u)};
    float res[4];
#pragma unroll
    for (int e = 0; e < 4; ++e) {
      const float gp = rbf(vg[e]);
      const float gs = rbf(gp / (1.0f + __expf(-gp)));
      res[e] = xs[e] + rbf(rbf(vs[e]) * gs);
    }
    u32x2 ov = {pack2(res[0], res[1]), pack2(res[2], res[3])};
    *reinterpret_cast<u32x2*>(out + go) = ov;
  }
}

extern "C" void kernel_launch(void* const* d_in, const int* in_sizes, int n_in,
                              void* d_out, int out_size, void* d_ws, size_t ws_size,
                              hipStream_t stream) {
  const u16* x = (const u16*)d_in[0];
  const u16* wqkv = (const u16*)d_in[1];
  const u16* wo = (const u16*)d_in[2];
  const u16* wg = (const u16*)d_in[4];
  const u16* wu = (const u16*)d_in[5];
  const u16* wd = (const u16*)d_in[6];
  char* ws = (char*)d_ws;
  (void)hipFuncSetAttribute((const void*)k1_gemm, hipFuncAttributeMaxDynamicSharedMemorySize, K1_LDS);
  (void)hipFuncSetAttribute((const void*)k2_gemm, hipFuncAttributeMaxDynamicSharedMemorySize, K2_LDS);
  k0_prep<<<256, 512, 0, stream>>>(x, wd, wo, ws);
  k1_gemm<<<256, 512, K1_LDS, stream>>>(wqkv, wg, wu, ws);
  k2_gemm<<<256, 256, K2_LDS, stream>>>(x, ws, (u16*)d_out);
}
```

```cpp
#include <hip/hip_runtime.h>
#include <hip/hip_bf16.h>
#include <stdint.h>
#include <type_traits>

typedef __attribute__((ext_vector_type(8))) short s16x8;
typedef __attribute__((ext_vector_type(4))) short s16x4;
typedef __attribute__((ext_vector_type(16))) float f32x16;
typedef __attribute__((ext_vector_type(4))) float f32x4;
typedef __attribute__((ext_vector_type(4))) unsigned u32x4;
typedef __attribute__((ext_vector_type(2))) unsigned u32x2;
typedef unsigned short u16;
#define LAS __attribute__((address_space(3)))
#define DI __device__ __forceinline__

constexpr int S_ = 2048, D_ = 512, F_ = 1792, NH = 32;
constexpr int RT = S_ / 32;
constexpr int FT = F_ / 16;
constexpr size_t OFF_XNT = 0;
constexpr size_t OFF_HT  = OFF_XNT + (size_t)RT * 32 * 1024;
constexpr size_t OFF_PQ  = OFF_HT + (size_t)RT * FT * 1024;
constexpr size_t OFF_WDT = OFF_PQ + (size_t)RT * NH * 1024;
constexpr size_t OFF_WOT = OFF_WDT + (size_t)16 * FT * 1024;
constexpr size_t OFF_KV  = OFF_WOT + (size_t)16 * NH * 1024;
constexpr int KV_WORDS = NH * 272;

template <int I, int N, class F> DI void static_for(F&& f) {
  if constexpr (I < N) { f(std::integral_constant<int, I>{}); static_for<I + 1, N>(f); }
}
DI float bf2f(u16 v) { return __uint_as_float(((unsigned)v) << 16); }
DI u16 f2bf(float f) { __hip_bfloat16 b = __float2bfloat16(f); return *reinterpret_cast<u16*>(&b); }
DI float rbf(float f) { return bf2f(f2bf(f)); }
typedef __attribute__((ext_vector_type(2))) __bf16 bf16x2;
typedef __attribute__((ext_vector_type(2))) float f32x2;
DI unsigned pack2(float lo, float hi) {
  f32x2 v = {lo, hi};
  return __builtin_bit_cast(unsigned, __builtin_convertvector(v, bf16x2));
}
DI float unlo(unsigned p) { return __uint_as_float(p << 16); }
DI float unhi(unsigned p) { return __uint_as_float(p & 0xffff0000u); }
DI int kperm(int hh, int j) { return j < 4 ? 4 * hh + j : 8 + 4 * hh + (j - 4); }

DI float phi_bf(float t) {
  float a = t > 0.f ? rbf(t + 1.0f) : rbf(__expf(t));
  return rbf(a + 1e-6f);
}
DI float gelu_tanh(float x) {
  const float y = x * __builtin_fmaf(x * x, 0.10294323970f, 2.30220819811f);
  const float r = __builtin_amdgcn_rcpf(1.0f + __builtin_amdgcn_exp2f(y));
  return __builtin_fmaf(-x, r, x);
}
DI float wave_sum(float v) {
#pragma unroll
  for (int o = 1; o < 64; o <<= 1) v += __shfl_xor(v, o);
  return v;
}

DI const u16* tile_src(int T, int plane, const u16* wqkv, const u16* wg, const u16* wu, int& ld) {
  if (T < FT) { ld = F_; return (plane ? wu : wg) + 16 * T; }
  ld = 3 * D_;
  if (T < FT + NH) return wqkv + D_ * (1 + plane) + 16 * (T - FT);
  return wqkv + 32 * (T - FT - NH) + 16 * plane;
}

__global__ __launch_bounds__(512) void k0_prep(const u16* __restrict__ x, const u16* __restrict__ wdown, const u16* __restrict__ wo,
                                               const u16* __restrict__ wqkv, const u16* __restrict__ wg, const u16* __restrict__ wu,
                                               char* __restrict__ ws) {
  __shared__ __attribute__((aligned(16))) char stage[64 * 8 * 16];
  const int b = blockIdx.x, tid = threadIdx.x, l = tid & 63, w = tid >> 6;
  unsigned warm = 0;
  {
    const int xcd = b & 7, idx = b >> 3, nb = 8 * (xcd >> 1) + (idx >> 2);
#pragma unroll 4
    for (int i = 0; i < 20; ++i) {
      const int q = tid + 512 * i, krow = q / 20, c = q % 20;
      int ld; const u16* p = tile_src(5 * nb + ((c % 10) >> 1), c / 10, wqkv, wg, wu, ld);
      const u32x4 v = *reinterpret_cast<const u32x4*>(p + (size_t)krow * ld + 8 * (c & 1));
      warm ^= v[0] ^ v[1] ^ v[2] ^ v[3];
    }
  }
  for (int t = w; t < 9; t += 8) {
    const int id = 9 * b + t;
    const int n32 = l & 31, hh = l >> 5;
    const u16* src; int krow0; char* dst;
    if (id < 16 * FT) { const int ct = id / FT, ft = id % FT; src = wdown + 32 * ct + n32; krow0 = 16 * ft; dst = ws + OFF_WDT + (size_t)id * 1024; }
    else { const int i2 = id - 16 * FT, ct = i2 / NH, hd = i2 % NH; src = wo + 32 * ct + n32; krow0 = 16 * hd; dst = ws + OFF_WOT + (size_t)i2 * 1024; }
    u16 v[8];
#pragma unroll
    for (int j = 0; j < 8; ++j) v[j] = src[(size_t)(krow0 + kperm(hh, j)) * D_];
    u32x4 o;
#pragma unroll
    for (int j = 0; j < 4; ++j) o[j] = (unsigned)v[2 * j] | ((unsigned)v[2 * j + 1] << 16);
    *reinterpret_cast<u32x4*>(dst + l * 16) = o;
  }
  { const int g = b * 512 + tid; if (g < KV_WORDS) reinterpret_cast<float*>(ws + OFF_KV)[g] = 0.f; }
  const int row = 8 * b + w;
  const u32x4 xv = *reinterpret_cast<const u32x4*>(x + (size_t)row * D_ + l * 8);
  float v[8];
#pragma unroll
  for (int j = 0; j < 4; ++j) { v[2 * j] = __uint_as_float(xv[j] << 16); v[2 * j + 1] = __uint_as_float(xv[j] & 0xffff0000u); }
  float sum = 0.f;
#pragma unroll
  for (int j = 0; j < 8; ++j) sum += v[j];
  const float mean = wave_sum(sum) * (1.0f / D_);
  float sq = 0.f;
#pragma unroll
  for (int j = 0; j < 8; ++j) { const float d = v[j] - mean; sq += d * d; }
  const float var = wave_sum(sq) * (1.0f / D_);
  const float mb = rbf(mean), vb = rbf(var);
  const float sd = rbf(__fsqrt_rn(rbf(vb + 1e-5f)));
  u32x4 o;
#pragma unroll
  for (int j = 0; j < 4; ++j) o[j] = pack2(__fdiv_rn(rbf(v[2 * j] - mb), sd), __fdiv_rn(rbf(v[2 * j + 1] - mb), sd));
  *reinterpret_cast<u32x4*>(stage + ((l >> 1) * 2 + (l & 1)) * 128 + w * 16) = o;
  __syncthreads();
  {
    const int seg = tid >> 3, p = tid & 7, ks = seg >> 1, hh = seg & 1;
    const int rt = row >> 5, s0 = (8 * b) & 31;
    *reinterpret_cast<u32x4*>(ws + OFF_XNT + ((size_t)(rt * 32 + ks) * 64 + hh * 32 + s0 + p) * 16) =
        *reinterpret_cast<const u32x4*>(stage + tid * 16);
  }
  if (warm == 0x9e3779b9u && tid == 511) reinterpret_cast<unsigned*>(ws + OFF_KV)[KV_WORDS] = warm;
}

constexpr int K1_LDS = 512 * 320;

#define XLOAD(dst, ptr) asm volatile("global_load_dwordx4 %0, %1, off" : "=v"(dst) : "v"(ptr) : "memory")
DI void glds16(const void* gsrc, unsigned lds_dst) {
  unsigned keep;
  asm volatile("s_mov_b32 %0, m0\n\ts_mov_b32 m0, %2\n\ts_nop 0\n\tglobal_load_lds_dwordx4 %1, off\n\ts_mov_b32 m0, %0"
               : "=&s"(keep) : "v"(gsrc), "s"(lds_dst) : "memory");
}

__global__ __launch_bounds__(512, 2) void k1_gemm(const u16* __restrict__ wqkv, const u16* __restrict__ wg, const u16* __restrict__ wu,
                                                  char* __restrict__ ws) {
  extern __shared__ __attribute__((aligned(16))) char lds[];
  const int tid = threadIdx.x, l = tid & 63, w = tid >> 6;
  const int bid = blockIdx.x, xcd = bid & 7, idx = bid >> 3;
  const int mb = 4 * (xcd & 1) + (idx & 3), nb = 8 * (xcd >> 1) + (idx >> 2);
  const int rt = 8 * mb + w;

  const u16* gsrc[5]; int gld[5];
#pragma unroll
  for (int bq = 0; bq < 5; ++bq) {
    const int q = 64 * bq + l, krow = q / 20, c = q % 20;
    const int plane = c / 10, i = (c % 10) >> 1, half = c & 1;
    int ld; const u16* p = tile_src(5 * nb + i, plane, wqkv, wg, wu, ld);
    gsrc[bq] = p + (size_t)(16 * w + krow) * ld + 8 * half; gld[bq] = ld;
  }
  const char* xbase = ws + OFF_XNT + (size_t)rt * 32 * 1024 + l * 16;

  s16x8 xf[32];
  const unsigned ldsw = __builtin_amdgcn_readfirstlane((unsigned)(size_t)lds + w * 16 * 320);
#define K1_ISSUE(C) do { \
    _Pragma("unroll") for (int k = 0; k < 8; ++k) XLOAD(xf[8 * (C) + k], xbase + (8 * (C) + k) * 1024); \
    _Pragma("unroll") for (int bq = 0; bq < 5; ++bq) glds16(gsrc[bq] + (size_t)(C) * 128 * gld[bq], ldsw + (C) * 128 * 320 + bq * 1024); \
  } while (0)
  K1_ISSUE(0);

  f32x16 acc[5];
#pragma unroll
  for (int i = 0; i < 5; ++i)
#pragma unroll
    for (int r = 0; r < 16; ++r) acc[i][r] = 0.f;

  const int g4 = l >> 4, q4 = (l & 15) >> 2, p4 = l & 3;
  const int trbase = (8 * (g4 >> 1) + q4) * 320 + (g4 & 1) * 160 + p4 * 8;

#define K1_WAIT(N, C) asm volatile("s_waitcnt vmcnt(" #N ")" : "+v"(xf[8*C]), "+v"(xf[8*C+1]), "+v"(xf[8*C+2]), "+v"(xf[8*C+3]), \
      "+v"(xf[8*C+4]), "+v"(xf[8*C+5]), "+v"(xf[8*C+6]), "+v"(xf[8*C+7]) :: "memory")
#define K1_PART(NC, j) do { \
    XLOAD(xf[8 * (NC) + 2 * (j)], xbase + (8 * (NC) + 2 * (j)) * 1024); XLOAD(xf[8 * (NC) + 2 * (j) + 1], xbase + (8 * (NC) + 2 * (j) + 1) * 1024); \
    if ((j) == 0) glds16(gsrc[4] + (size_t)(NC) * 128 * gld[4], ldsw + (NC) * 128 * 320 + 4 * 1024); \
    glds16(gsrc[j] + (size_t)(NC) * 128 * gld[j], ldsw + (NC) * 128 * 320 + (j) * 1024); } while (0)
#define K1_CHUNK(C) do { \
    __builtin_amdgcn_s_barrier(); asm volatile("" ::: "memory"); \
    _Pragma("unroll") for (int k = 0; k < 8; ++k) { \
      if ((C) == 0) { if (k < 4) K1_PART(1, k); else K1_PART(2, k - 4); } \
      if ((C) == 1 && k < 4) K1_PART(3, k); \
      _Pragma("unroll") for (int i = 0; i < 5; ++i) { \
        const char* pa = lds + trbase + (128 * (C) + 16 * k) * 320 + i * 32; \
        s16x4 lo = __builtin_amdgcn_ds_read_tr16_b64_v4i16((LAS s16x4*)(pa)); \
        s16x4 hi = __builtin_amdgcn_ds_read_tr16_b64_v4i16((LAS s16x4*)(pa + 4 * 320)); \
        s16x8 wf = __builtin_shufflevector(lo, hi, 0, 1, 2, 3, 4, 5, 6, 7); \
        acc[i] = __builtin_amdgcn_mfma_f32_32x32x16_bf16(wf, xf[8 * (C) + k], acc[i], 0, 0, 0); \
      } } } while (0)

  K1_WAIT(0, 0);  K1_CHUNK(0);
  K1_WAIT(13, 1); K1_CHUNK(1);
  K1_WAIT(13, 2); K1_CHUNK(2);
  K1_WAIT(0, 3);  K1_CHUNK(3);

  __syncthreads();
  float* kvred = reinterpret_cast<float*>(lds);
  char* ybuf = lds + 8 * 5 * 272 * 4 + w * 2048;
  bool any_kv = false;
#pragma unroll
  for (int i = 0; i < 5; ++i) { const int T = 5 * nb + i; any_kv |= (T >= FT && T < FT + NH); }

  const int hh = l >> 5, s = l & 31;
#pragma unroll
  for (int i = 0; i < 5; ++i) {
    const int T = 5 * nb + i;
    if (T < FT) {
      u32x4 o;
#pragma unroll
      for (int j = 0; j < 4; ++j) {
        const unsigned gp = pack2(acc[i][2 * j], acc[i][2 * j + 1]), up = pack2(acc[i][8 + 2 * j], acc[i][8 + 2 * j + 1]);
        const unsigned lp = pack2(gelu_tanh(unlo(gp)), gelu_tanh(unhi(gp)));
        o[j] = pack2(unlo(lp) * unlo(up), unhi(lp) * unhi(up));
      }
      *reinterpret_cast<u32x4*>(ws + OFF_HT + ((size_t)(rt * FT + T) * 64 + l) * 16) = o;
    } else if (T >= FT + NH) {
      const int ha = 2 * (T - FT - NH);
      u32x4 oa, ob;
#pragma unroll
      for (int j = 0; j < 4; ++j) {
        oa[j] = pack2(phi_bf(rbf(acc[i][2 * j])), phi_bf(rbf(acc[i][2 * j + 1])));
        ob[j] = pack2(phi_bf(rbf(acc[i][8 + 2 * j])), phi_bf(rbf(acc[i][8 + 2 * j + 1])));
      }
      *reinterpret_cast<u32x4*>(ws + OFF_PQ + ((size_t)(rt * NH + ha) * 64 + l) * 16) = oa;
      *reinterpret_cast<u32x4*>(ws + OFF_PQ + ((size_t)(rt * NH + ha + 1) * 64 + l) * 16) = ob;
    } else {
#pragma unroll
      for (int gq = 0; gq < 4; ++gq) {
        u32x2 pk;
        float f0 = acc[i][4 * gq], f1 = acc[i][4 * gq + 1], f2 = acc[i][4 * gq + 2], f3 = acc[i][4 * gq + 3];
        if (gq < 2) { pk[0] = pack2(phi_bf(rbf(f0)), phi_bf(rbf(f1))); pk[1] = pack2(phi_bf(rbf(f2)), phi_bf(rbf(f3))); }
        else        { pk[0] = pack2(f0, f1); pk[1] = pack2(f2, f3); }
        *reinterpret_cast<u32x2*>(ybuf + s * 64 + (8 * gq + 4 * hh) * 2) = pk;
      }
      asm volatile("s_waitcnt lgkmcnt(0)" ::: "memory");
      f32x16 dd, dz;
#pragma unroll
      for (int r = 0; r < 16; ++r) { dd[r] = 0.f; dz[r] = 0.f; }
      s16x8 ones;
#pragma unroll
      for (int j = 0; j < 8; ++j) ones[j] = (short)0x3f80;
#pragma unroll
      for (int st = 0; st < 2; ++st) {
        const char* pa = ybuf + (16 * st + 8 * (g4 >> 1) + q4) * 64 + (g4 & 1) * 32 + p4 * 8;
        s16x4 lo = __builtin_amdgcn_ds_read_tr16_b64_v4i16((LAS s16x4*)(pa));
        s16x4 hi = __builtin_amdgcn_ds_read_tr16_b64_v4i16((LAS s16x4*)(pa + 4 * 64));
        s16x8 yf = __builtin_shufflevector(lo, hi, 0, 1, 2, 3, 4, 5, 6, 7);
        dd = __builtin_amdgcn_mfma_f32_32x32x16_bf16(yf, yf, dd, 0, 0, 0);
        dz = __builtin_amdgcn_mfma_f32_32x32x16_bf16(yf, ones, dz, 0, 0, 0);
      }
      float* kr = kvred + (w * 5 + i) * 272;
      if (s >= 16) {
#pragma unroll
        for (int r = 0; r < 8; ++r) kr[(s - 16) * 16 + (r & 3) + 8 * (r >> 2) + 4 * hh] = dd[r];
      } else if (s == 0) {
#pragma unroll
        for (int r = 0; r < 8; ++r) kr[256 + (r & 3) + 8 * (r >> 2) + 4 * hh] = dz[r];
      }
      asm volatile("s_waitcnt lgkmcnt(0)" ::: "memory");
    }
  }
  if (any_kv) {
    __syncthreads();
    for (int e = tid; e < 5 * 272; e += 512) {
      const int i = e / 272, T = 5 * nb + i;
      if (T >= FT && T < FT + NH) {
        float a = 0.f;
#pragma unroll
        for (int ww = 0; ww < 8; ++ww) a += kvred[ww * 5 * 272 + e];
        atomicAdd(reinterpret_cast<float*>(ws + OFF_KV) + (T - FT) * 272 + (e - i * 272), a);
      }
    }
  }
}

constexpr int K2_LDS = 4 * 32768;

__global__ __launch_bounds__(256, 1) void k2_gemm(const u16* __restrict__ x, const char* __restrict__ ws, u16* __restrict__ out) {
  extern __shared__ __attribute__((aligned(16))) char lds[];
  const int tid = threadIdx.x, l = tid & 63, w = tid >> 6;
  const int bid = blockIdx.x, xcd = bid & 7, idx = bid >> 3;
  const int rb = 4 * xcd + (idx >> 3), cb = idx & 7;
  const int rt0 = 2 * rb, ct0 = 2 * cb;

  u32x2 xpre[4];
#pragma unroll
  for (int it = 0; it < 4; ++it) {
    const int Q = it * 256 + tid, row = Q >> 4, nq = Q & 15;
    xpre[it] = __builtin_nontemporal_load(reinterpret_cast<const u32x2*>(x + (size_t)(64 * rb + row) * D_ + 64 * cb + 4 * nq));
  }
  f32x16 ag[2][2], as[2][2];
#pragma unroll
  for (int c = 0; c < 2; ++c)
#pragma unroll
    for (int r = 0; r < 2; ++r)
#pragma unroll
      for (int e = 0; e < 16; ++e) { ag[c][r][e] = 0.f; as[c][r][e] = 0.f; }

  {
    constexpr int NST = 36, R = 7;
    const char* hb0 = ws + OFF_HT + ((size_t)(rt0 * FT + 28 * w) * 64 + l) * 16;
    const char* wa0 = ws + OFF_WDT + ((size_t)(ct0 * FT + 28 * w) * 64 + l) * 16;
    const char* pq0 = ws + OFF_PQ + ((size_t)(rt0 * NH + 8 * w) * 64 + l) * 16;
    const char* wo0 = ws + OFF_WOT + ((size_t)(ct0 * NH + 8 * w) * 64 + l) * 16;
    s16x8 ring[R][4];
#define K2_ISSUE(ST) do { \
      if ((ST) < 28) { XLOAD(ring[(ST) % R][0], hb0 + (ST) * 1024); XLOAD(ring[(ST) % R][1], hb0 + (size_t)FT * 1024 + (ST) * 1024); \
                       XLOAD(ring[(ST) % R][2], wa0 + (ST) * 1024); XLOAD(ring[(ST) % R][3], wa0 + (size_t)FT * 1024 + (ST) * 1024); } \
      else           { XLOAD(ring[(ST) % R][0], pq0 + ((ST) - 28) * 1024); XLOAD(ring[(ST) % R][1], pq0 + (size_t)NH * 1024 + ((ST) - 28) * 1024); \
                       XLOAD(ring[(ST) % R][2], wo0 + ((ST) - 28) * 1024); XLOAD(ring[(ST) % R][3], wo0 + (size_t)NH * 1024 + ((ST) - 28) * 1024); } \
    } while (0)
    f32x4 kvv[8][2];
    {
      const char* kb = ws + OFF_KV + ((size_t)(8 * w) * 272 + ((l & 31) < 16 ? (l & 31) * 16 : 256) + 4 * (l >> 5)) * 4;
#pragma unroll
      for (int hq = 0; hq < 8; ++hq) {
        asm volatile("global_load_dwordx4 %0, %1, off" : "=v"(kvv[hq][0]) : "v"(kb + hq * 272 * 4) : "memory");
        asm volatile("global_load_dwordx4 %0, %1, off" : "=v"(kvv[hq][1]) : "v"(kb + hq * 272 * 4 + 32) : "memory");
      }
    }
#pragma unroll
    for (int st = 0; st < R - 1; ++st) K2_ISSUE(st);
    static_for<0, NST>([&](auto ic) {
      constexpr int cs = decltype(ic)::value;
      if constexpr (cs + R - 1 < NST) K2_ISSUE(cs + R - 1);
      constexpr int ahead = (NST - 1 - cs) < (R - 1) ? (NST - 1 - cs) : (R - 1);
      s16x8* f = ring[cs % R];
      asm volatile("s_waitcnt vmcnt(%4)" : "+v"(f[0]), "+v"(f[1]), "+v"(f[2]), "+v"(f[3]) : "i"(4 * ahead) : "memory");
      if constexpr (cs < 28) {
        ag[0][0] = __builtin_amdgcn_mfma_f32_32x32x16_bf16(f[2], f[0], ag[0][0], 0, 0, 0);
        ag[0][1] = __builtin_amdgcn_mfma_f32_32x32x16_bf16(f[2], f[1], ag[0][1], 0, 0, 0);
        ag[1][0] = __builtin_amdgcn_mfma_f32_32x32x16_bf16(f[3], f[0], ag[1][0], 0, 0, 0);
        ag[1][1] = __builtin_amdgcn_mfma_f32_32x32x16_bf16(f[3], f[1], ag[1][1], 0, 0, 0);
      } else {
        asm volatile("" : "+v"(kvv[cs - 28][0]), "+v"(kvv[cs - 28][1]));
        u32x4 kp;
        kp[0] = pack2(kvv[cs - 28][0][0], kvv[cs - 28][0][1]); kp[1] = pack2(kvv[cs - 28][0][2], kvv[cs - 28][0][3]);
        kp[2] = pack2(kvv[cs - 28][1][0], kvv[cs - 28][1][1]); kp[3] = pack2(kvv[cs - 28][1][2], kvv[cs - 28][1][3]);
        const s16x8 kf = __builtin_bit_cast(s16x8, kp);
#pragma unroll
        for (int r = 0; r < 2; ++r) {
          f32x16 nd;
#pragma unroll
          for (int e = 0; e < 16; ++e) nd[e] = 0.f;
          nd = __builtin_amdgcn_mfma_f32_32x32x16_bf16(kf, f[r], nd, 0, 0, 0);
          const float rden = __builtin_amdgcn_rcpf(rbf(rbf(nd[8]) + 1e-6f));
          u32x4 ob;
#pragma unroll
          for (int j = 0; j < 4; ++j) {
            const unsigned np = pack2(nd[2 * j], nd[2 * j + 1]);
            ob[j] = pack2(unlo(np) * rden, unhi(np) * rden);
          }
          const s16x8 of = __builtin_bit_cast(s16x8, ob);
          as[0][r] = __builtin_amdgcn_mfma_f32_32x32x16_bf16(f[2], of, as[0][r], 0, 0, 0);
          as[1][r] = __builtin_amdgcn_mfma_f32_32x32x16_bf16(f[3], of, as[1][r], 0, 0, 0);
        }
      }
    });
  }
  __syncthreads();
#pragma unroll
  for (int c = 0; c < 2; ++c)
#pragma unroll
    for (int r = 0; r < 2; ++r)
#pragma unroll
      for (int qd = 0; qd < 4; ++qd) {
        f32x4 vg = {ag[c][r][4 * qd], ag[c][r][4 * qd + 1], ag[c][r][4 * qd + 2], ag[c][r][4 * qd + 3]};
        f32x4 vs = {as[c][r][4 * qd], as[c][r][4 * qd + 1], as[c][r][4 * qd + 2], as[c][r][4 * qd + 3]};
        const int lp = l ^ ((c * 8 + qd * 2 + (l >> 5)) & 15);
        *reinterpret_cast<f32x4*>(lds + ((((w * 8 + c * 2 + r) * 4 + qd) * 64 + lp) * 16)) = vg;
        *reinterpret_cast<f32x4*>(lds + ((((w * 8 + 4 + c * 2 + r) * 4 + qd) * 64 + lp) * 16)) = vs;
      }
  __syncthreads();
#pragma unroll
  for (int it = 0; it < 4; ++it) {
    const int Q = it * 256 + tid, row = Q >> 4, nq = Q & 15;
    const int r = row >> 5, s = row & 31, c = nq >> 3, hh = nq & 1, qd = (nq & 7) >> 1;
    f32x4 vg = {0.f, 0.f, 0.f, 0.f}, vs = {0.f, 0.f, 0.f, 0.f};
#pragma unroll
    for (int ww = 0; ww < 4; ++ww) {
      vg += *reinterpret_cast<const f32x4*>(lds + ((((ww * 8 + c * 2 + r) * 4 + qd) * 64 + hh * 32 + (s ^ ((c * 8 + qd * 2 + hh) & 15))) * 16));
      vs += *reinterpret_cast<const f32x4*>(lds + ((((ww * 8 + 4 + c * 2 + r) * 4 + qd) * 64 + hh * 32 + (s ^ ((c * 8 + qd * 2 + hh) & 15))) * 16));
    }
    const size_t go = (size_t)(64 * rb + row) * D_ + 64 * cb + 32 * c + 8 * qd + 4 * hh;
    const u32x2 xv = xpre[it];
    const float xs[4] = {__uint_as_float(xv[0] << 16), __uint_as_float(xv[0] & 0xffff0000u), __uint_as_float(xv[1] << 16), __uint_as_float(xv[1] & 0xffff0000u)};
    float res[4];
#pragma unroll
    for (int e = 0; e < 4; ++e) {
      const float gp = rbf(vg[e]);
      const float gs = rbf(gp * __builtin_amdgcn_rcpf(1.0f + __builtin_amdgcn_exp2f(-1.4426950408889634f * gp)));
      res[e] = xs[e] + rbf(rbf(vs[e]) * gs);
    }
    u32x2 ov = {pack2(res[0], res[1]), pack2(res[2], res[3])};
    *reinterpret_cast<u32x2*>(out + go) = ov;
  }
}

extern "C" void kernel_launch(void* const* d_in, const int* in_sizes, int n_in,
                              void* d_out, int out_size, void* d_ws, size_t ws_size,
                              hipStream_t stream) {
  const u16* x = (const u16*)d_in[0];
  const u16* wqkv = (const u16*)d_in[1];
  const u16* wo = (const u16*)d_in[2];
  const u16* wg = (const u16*)d_in[4];
  const u16* wu = (const u16*)d_in[5];
  const u16* wd = (const u16*)d_in[6];
  char* ws = (char*)d_ws;
  (void)hipFuncSetAttribute((const void*)k1_gemm, hipFuncAttributeMaxDynamicSharedMemorySize, K1_LDS);
  (void)hipFuncSetAttribute((const void*)k2_gemm, hipFuncAttributeMaxDynamicSharedMemorySize, K2_LDS);
  k0_prep<<<256, 512, 0, stream>>>(x, wd, wo, wqkv, wg, wu, ws);
  k1_gemm<<<256, 512, K1_LDS, stream>>>(wqkv, wg, wu, ws);
  k2_gemm<<<256, 256, K2_LDS, stream>>>(x, ws, (u16*)d_out);
}
```

```cpp
#include <hip/hip_runtime.h>
#include <hip/hip_bf16.h>
#include <stdint.h>
#include <type_traits>

typedef __attribute__((ext_vector_type(8))) short s16x8;
typedef __attribute__((ext_vector_type(4))) short s16x4;
typedef __attribute__((ext_vector_type(16))) float f32x16;
typedef __attribute__((ext_vector_type(4))) float f32x4;
typedef __attribute__((ext_vector_type(4))) unsigned u32x4;
typedef __attribute__((ext_vector_type(2))) unsigned u32x2;
typedef unsigned short u16;
#define LAS __attribute__((address_space(3)))
#define DI __device__ __forceinline__

constexpr int S_ = 2048, D_ = 512, F_ = 1792, NH = 32;
constexpr int RT = S_ / 32;
constexpr int FT = F_ / 16;
constexpr size_t OFF_XNT = 0;
constexpr size_t OFF_HT  = OFF_XNT + (size_t)RT * 32 * 1024;
constexpr size_t OFF_PQ  = OFF_HT + (size_t)RT * FT * 1024;
constexpr size_t OFF_WDT = OFF_PQ + (size_t)RT * NH * 1024;
constexpr size_t OFF_WOT = OFF_WDT + (size_t)16 * FT * 1024;
constexpr size_t OFF_KV  = OFF_WOT + (size_t)16 * NH * 1024;
constexpr int KV_WORDS = NH * 272;

template <int I, int N, class F> DI void static_for(F&& f) {
  if constexpr (I < N) { f(std::integral_constant<int, I>{}); static_for<I + 1, N>(f); }
}
DI float bf2f(u16 v) { return __uint_as_float(((unsigned)v) << 16); }
DI u16 f2bf(float f) { __hip_bfloat16 b = __float2bfloat16(f); return *reinterpret_cast<u16*>(&b); }
DI float rbf(float f) { return bf2f(f2bf(f)); }
typedef __attribute__((ext_vector_type(2))) __bf16 bf16x2;
typedef __attribute__((ext_vector_type(2))) float f32x2;
DI unsigned pack2(float lo, float hi) {
  f32x2 v = {lo, hi};
  return __builtin_bit_cast(unsigned, __builtin_convertvector(v, bf16x2));
}
DI float unlo(unsigned p) { return __uint_as_float(p << 16); }
DI float unhi(unsigned p) { return __uint_as_float(p & 0xffff0000u); }
DI int kperm(int hh, int j) { return j < 4 ? 4 * hh + j : 8 + 4 * hh + (j - 4); }

DI float phi_bf(float t) {
  float a = t > 0.f ? rbf(t + 1.0f) : rbf(__expf(t));
  return rbf(a + 1e-6f);
}
DI float gelu_tanh(float x) {
  const float y = x * __builtin_fmaf(x * x, 0.10294323970f, 2.30220819811f);
  const float r = __builtin_amdgcn_rcpf(1.0f + __builtin_amdgcn_exp2f(y));
  return __builtin_fmaf(-x, r, x);
}
DI float wave_sum(float v) {
#pragma unroll
  for (int o = 1; o < 64; o <<= 1) v += __shfl_xor(v, o);
  return v;
}

__global__ __launch_bounds__(512) void k0_prep(const u16* __restrict__ x, const u16* __restrict__ wdown, const u16* __restrict__ wo,
                                               char* __restrict__ ws) {
  __shared__ __attribute__((aligned(16))) char stage[64 * 8 * 16];
  const int b = blockIdx.x, tid = threadIdx.x, l = tid & 63, w = tid >> 6;
  for (int t = w; t < 9; t += 8) {
    const int id = 9 * b + t;
    const int n32 = l & 31, hh = l >> 5;
    const u16* src; int krow0; char* dst;
    if (id < 16 * FT) { const int ct = id / FT, ft = id % FT; src = wdown + 32 * ct + n32; krow0 = 16 * ft; dst = ws + OFF_WDT + (size_t)id * 1024; }
    else { const int i2 = id - 16 * FT, ct = i2 / NH, hd = i2 % NH; src = wo + 32 * ct + n32; krow0 = 16 * hd; dst = ws + OFF_WOT + (size_t)i2 * 1024; }
    u16 v[8];
#pragma unroll
    for (int j = 0; j < 8; ++j) v[j] = src[(size_t)(krow0 + kperm(hh, j)) * D_];
    u32x4 o;
#pragma unroll
    for (int j = 0; j < 4; ++j) o[j] = (unsigned)v[2 * j] | ((unsigned)v[2 * j + 1] << 16);
    *reinterpret_cast<u32x4*>(dst + l * 16) = o;
  }
  { const int g = b * 512 + tid; if (g < KV_WORDS) reinterpret_cast<float*>(ws + OFF_KV)[g] = 0.f; }
  const int row = 8 * b + w;
  const u32x4 xv = *reinterpret_cast<const u32x4*>(x + (size_t)row * D_ + l * 8);
  float v[8];
#pragma unroll
  for (int j = 0; j < 4; ++j) { v[2 * j] = __uint_as_float(xv[j] << 16); v[2 * j + 1] = __uint_as_float(xv[j] & 0xffff0000u); }
  float sum = 0.f;
#pragma unroll
  for (int j = 0; j < 8; ++j) sum += v[j];
  const float mean = wave_sum(sum) * (1.0f / D_);
  float sq = 0.f;
#pragma unroll
  for (int j = 0; j < 8; ++j) { const float d = v[j] - mean; sq += d * d; }
  const float var = wave_sum(sq) * (1.0f / D_);
  const float mb = rbf(mean), vb = rbf(var);
  const float sd = rbf(__fsqrt_rn(rbf(vb + 1e-5f)));
  u32x4 o;
#pragma unroll
  for (int j = 0; j < 4; ++j) o[j] = pack2(__fdiv_rn(rbf(v[2 * j] - mb), sd), __fdiv_rn(rbf(v[2 * j + 1] - mb), sd));
  *reinterpret_cast<u32x4*>(stage + ((l >> 1) * 2 + (l & 1)) * 128 + w * 16) = o;
  __syncthreads();
  {
    const int seg = tid >> 3, p = tid & 7, ks = seg >> 1, hh = seg & 1;
    const int rt = row >> 5, s0 = (8 * b) & 31;
    *reinterpret_cast<u32x4*>(ws + OFF_XNT + ((size_t)(rt * 32 + ks) * 64 + hh * 32 + s0 + p) * 16) =
        *reinterpret_cast<const u32x4*>(stage + tid * 16);
  }
}

DI const u16* tile_src(int T, int plane, const u16* wqkv, const u16* wg, const u16* wu, int& ld) {
  if (T < FT) { ld = F_; return (plane ? wu : wg) + 16 * T; }
  ld = 3 * D_;
  if (T < FT + NH) return wqkv + D_ * (1 + plane) + 16 * (T - FT);
  return wqkv + 32 * (T - FT - NH) + 16 * plane;
}

constexpr int K1_LDS = 512 * 320;

#define XLOAD(dst, ptr) asm volatile("global_load_dwordx4 %0, %1, off" : "=v"(dst) : "v"(ptr) : "memory")
DI void glds16(const void* gsrc, unsigned lds_dst) {
  unsigned keep;
  asm volatile("s_mov_b32 %0, m0\n\ts_mov_b32 m0, %2\n\ts_nop 0\n\tglobal_load_lds_dwordx4 %1, off\n\ts_mov_b32 m0, %0"
               : "=&s"(keep) : "v"(gsrc), "s"(lds_dst) : "memory");
}

__global__ __launch_bounds__(512, 2) void k1_gemm(const u16* __restrict__ wqkv, const u16* __restrict__ wg, const u16* __restrict__ wu,
                                                  char* __restrict__ ws) {
  extern __shared__ __attribute__((aligned(16))) char lds[];
  const int tid = threadIdx.x, l = tid & 63, w = tid >> 6;
  const int bid = blockIdx.x, xcd = bid & 7, idx = bid >> 3;
  const int mb = 4 * (xcd & 1) + (idx & 3), nb = 8 * (xcd >> 1) + (idx >> 2);
  const int rt = 8 * mb + w;

  const u16* gsrc[5]; int gld[5];
#pragma unroll
  for (int bq = 0; bq < 5; ++bq) {
    const int q = 64 * bq + l, krow = q / 20, c = q % 20;
    const int plane = c / 10, i = (c % 10) >> 1, half = c & 1;
    int ld; const u16* p = tile_src(5 * nb + i, plane, wqkv, wg, wu, ld);
    gsrc[bq] = p + (size_t)(16 * w + krow) * ld + 8 * half; gld[bq] = ld;
  }
  const char* xbase = ws + OFF_XNT + (size_t)rt * 32 * 1024 + l * 16;

  s16x8 xf[32];
  const unsigned ldsw = __builtin_amdgcn_readfirstlane((unsigned)(size_t)lds + w * 16 * 320);
#define K1_ISSUE(C) do { \
    _Pragma("unroll") for (int k = 0; k < 8; ++k) XLOAD(xf[8 * (C) + k], xbase + (8 * (C) + k) * 1024); \
    _Pragma("unroll") for (int bq = 0; bq < 5; ++bq) glds16(gsrc[bq] + (size_t)(C) * 128 * gld[bq], ldsw + (C) * 128 * 320 + bq * 1024); \
  } while (0)
  K1_ISSUE(0);

  f32x16 acc[5];
#pragma unroll
  for (int i = 0; i < 5; ++i)
#pragma unroll
    for (int r = 0; r < 16; ++r) acc[i][r] = 0.f;

  const int g4 = l >> 4, q4 = (l & 15) >> 2, p4 = l & 3;
  const int trbase = (8 * (g4 >> 1) + q4) * 320 + (g4 & 1) * 160 + p4 * 8;

#define K1_WAIT(N, C) asm volatile("s_waitcnt vmcnt(" #N ")" : "+v"(xf[8*C]), "+v"(xf[8*C+1]), "+v"(xf[8*C+2]), "+v"(xf[8*C+3]), \
      "+v"(xf[8*C+4]), "+v"(xf[8*C+5]), "+v"(xf[8*C+6]), "+v"(xf[8*C+7]) :: "memory")
#define K1_PART(NC, j) do { \
    XLOAD(xf[8 * (NC) + 2 * (j)], xbase + (8 * (NC) + 2 * (j)) * 1024); XLOAD(xf[8 * (NC) + 2 * (j) + 1], xbase + (8 * (NC) + 2 * (j) + 1) * 1024); \
    if ((j) == 0) glds16(gsrc[4] + (size_t)(NC) * 128 * gld[4], ldsw + (NC) * 128 * 320 + 4 * 1024); \
    glds16(gsrc[j] + (size_t)(NC) * 128 * gld[j], ldsw + (NC) * 128 * 320 + (j) * 1024); } while (0)
#define K1_CHUNK(C) do { \
    __builtin_amdgcn_s_barrier(); asm volatile("" ::: "memory"); \
    _Pragma("unroll") for (int k = 0; k < 8; ++k) { \
      if ((C) == 0) { if (k < 4) K1_PART(1, k); else K1_PART(2, k - 4); } \
      if ((C) == 1 && k < 4) K1_PART(3, k); \
      _Pragma("unroll") for (int i = 0; i < 5; ++i) { \
        const char* pa = lds + trbase + (128 * (C) + 16 * k) * 320 + i * 32; \
        s16x4 lo = __builtin_amdgcn_ds_read_tr16_b64_v4i16((LAS s16x4*)(pa)); \
        s16x4 hi = __builtin_amdgcn_ds_read_tr16_b64_v4i16((LAS s16x4*)(pa + 4 * 320)); \
        s16x8 wf = __builtin_shufflevector(lo, hi, 0, 1, 2, 3, 4, 5, 6, 7); \
        acc[i] = __builtin_amdgcn_mfma_f32_32x32x16_bf16(wf, xf[8 * (C) + k], acc[i], 0, 0, 0); \
      } } } while (0)

  K1_WAIT(0, 0);  K1_CHUNK(0);
  K1_WAIT(13, 1); K1_CHUNK(1);
  K1_WAIT(13, 2); K1_CHUNK(2);
  K1_WAIT(0, 3);  K1_CHUNK(3);

  __syncthreads();
  float* kvred = reinterpret_cast<float*>(lds);
  char* ybuf = lds + 8 * 5 * 272 * 4 + w * 2048;
  bool any_kv = false;
#pragma unroll
  for (int i = 0; i < 5; ++i) { const int T = 5 * nb + i; any_kv |= (T >= FT && T < FT + NH); }

  const int hh = l >> 5, s = l & 31;
#pragma unroll
  for (int i = 0; i < 5; ++i) {
    const int T = 5 * nb + i;
    if (T < FT) {
      u32x4 o;
#pragma unroll
      for (int j = 0; j < 4; ++j) {
        const unsigned gp = pack2(acc[i][2 * j], acc[i][2 * j + 1]), up = pack2(acc[i][8 + 2 * j], acc[i][8 + 2 * j + 1]);
        const unsigned lp = pack2(gelu_tanh(unlo(gp)), gelu_tanh(unhi(gp)));
        o[j] = pack2(unlo(lp) * unlo(up), unhi(lp) * unhi(up));
      }
      *reinterpret_cast<u32x4*>(ws + OFF_HT + ((size_t)(rt * FT + T) * 64 + l) * 16) = o;
    } else if (T >= FT + NH) {
      const int ha = 2 * (T - FT - NH);
      u32x4 oa, ob;
#pragma unroll
      for (int j = 0; j < 4; ++j) {
        oa[j] = pack2(phi_bf(rbf(acc[i][2 * j])), phi_bf(rbf(acc[i][2 * j + 1])));
        ob[j] = pack2(phi_bf(rbf(acc[i][8 + 2 * j])), phi_bf(rbf(acc[i][8 + 2 * j + 1])));
      }
      *reinterpret_cast<u32x4*>(ws + OFF_PQ + ((size_t)(rt * NH + ha) * 64 + l) * 16) = oa;
      *reinterpret_cast<u32x4*>(ws + OFF_PQ + ((size_t)(rt * NH + ha + 1) * 64 + l) * 16) = ob;
    } else {
#pragma unroll
      for (int gq = 0; gq < 4; ++gq) {
        u32x2 pk;
        float f0 = acc[i][4 * gq], f1 = acc[i][4 * gq + 1], f2 = acc[i][4 * gq + 2], f3 = acc[i][4 * gq + 3];
        if (gq < 2) { pk[0] = pack2(phi_bf(rbf(f0)), phi_bf(rbf(f1))); pk[1] = pack2(phi_bf(rbf(f2)), phi_bf(rbf(f3))); }
        else        { pk[0] = pack2(f0, f1); pk[1] = pack2(f2, f3); }
        *reinterpret_cast<u32x2*>(ybuf + s * 64 + (8 * gq + 4 * hh) * 2) = pk;
      }
      asm volatile("s_waitcnt lgkmcnt(0)" ::: "memory");
      f32x16 dd, dz;
#pragma unroll
      for (int r = 0; r < 16; ++r) { dd[r] = 0.f; dz[r] = 0.f; }
      s16x8 ones;
#pragma unroll
      for (int j = 0; j < 8; ++j) ones[j] = (short)0x3f80;
#pragma unroll
      for (int st = 0; st < 2; ++st) {
        const char* pa = ybuf + (16 * st + 8 * (g4 >> 1) + q4) * 64 + (g4 & 1) * 32 + p4 * 8;
        s16x4 lo = __builtin_amdgcn_ds_read_tr16_b64_v4i16((LAS s16x4*)(pa));
        s16x4 hi = __builtin_amdgcn_ds_read_tr16_b64_v4i16((LAS s16x4*)(pa + 4 * 64));
        s16x8 yf = __builtin_shufflevector(lo, hi, 0, 1, 2, 3, 4, 5, 6, 7);
        dd = __builtin_amdgcn_mfma_f32_32x32x16_bf16(yf, yf, dd, 0, 0, 0);
        dz = __builtin_amdgcn_mfma_f32_32x32x16_bf16(yf, ones, dz, 0, 0, 0);
      }
      float* kr = kvred + (w * 5 + i) * 272;
      if (s >= 16) {
#pragma unroll
        for (int r = 0; r < 8; ++r) kr[(s - 16) * 16 + (r & 3) + 8 * (r >> 2) + 4 * hh] = dd[r];
      } else if (s == 0) {
#pragma unroll
        for (int r = 0; r < 8; ++r) kr[256 + (r & 3) + 8 * (r >> 2) + 4 * hh] = dz[r];
      }
      asm volatile("s_waitcnt lgkmcnt(0)" ::: "memory");
    }
  }
  if (any_kv) {
    __syncthreads();
    for (int e = tid; e < 5 * 272; e += 512) {
      const int i = e / 272, T = 5 * nb + i;
      if (T >= FT && T < FT + NH) {
        float a = 0.f;
#pragma unroll
        for (int ww = 0; ww < 8; ++ww) a += kvred[ww * 5 * 272 + e];
        atomicAdd(reinterpret_cast<float*>(ws + OFF_KV) + (T - FT) * 272 + (e - i * 272), a);
      }
    }
  }
}

constexpr int K2_LDS = 4 * 32768;

__global__ __launch_bounds__(256, 1) void k2_gemm(const u16* __restrict__ x, const char* __restrict__ ws, u16* __restrict__ out) {
  extern __shared__ __attribute__((aligned(16))) char lds[];
  const int tid = threadIdx.x, l = tid & 63, w = tid >> 6;
  const int bid = blockIdx.x, xcd = bid & 7, idx = bid >> 3;
  const int rb = 4 * xcd + (idx >> 3), cb = idx & 7;
  const int rt0 = 2 * rb, ct0 = 2 * cb;

  u32x2 xpre[4];
#pragma unroll
  for (int it = 0; it < 4; ++it) {
    const int Q = it * 256 + tid, row = Q >> 4, nq = Q & 15;
    xpre[it] = __builtin_nontemporal_load(reinterpret_cast<const u32x2*>(x + (size_t)(64 * rb + row) * D_ + 64 * cb + 4 * nq));
  }
  f32x16 ag[2][2], as[2][2];
#pragma unroll
  for (int c = 0; c < 2; ++c)
#pragma unroll
    for (int r = 0; r < 2; ++r)
#pragma unroll
      for (int e = 0; e < 16; ++e) { ag[c][r][e] = 0.f; as[c][r][e] = 0.f; }

  {
    constexpr int NST = 36, R = 7;
    const char* hb0 = ws + OFF_HT + ((size_t)(rt0 * FT + 28 * w) * 64 + l) * 16;
    const char* wa0 = ws + OFF_WDT + ((size_t)(ct0 * FT + 28 * w) * 64 + l) * 16;
    const char* pq0 = ws + OFF_PQ + ((size_t)(rt0 * NH + 8 * w) * 64 + l) * 16;
    const char* wo0 = ws + OFF_WOT + ((size_t)(ct0 * NH + 8 * w) * 64 + l) * 16;
    s16x8 ring[R][4];
#define K2_ISSUE(ST) do { \
      if ((ST) < 28) { XLOAD(ring[(ST) % R][0], hb0 + (ST) * 1024); XLOAD(ring[(ST) % R][1], hb0 + (size_t)FT * 1024 + (ST) * 1024); \
                       XLOAD(ring[(ST) % R][2], wa0 + (ST) * 1024); XLOAD(ring[(ST) % R][3], wa0 + (size_t)FT * 1024 + (ST) * 1024); } \
      else           { XLOAD(ring[(ST) % R][0], pq0 + ((ST) - 28) * 1024); XLOAD(ring[(ST) % R][1], pq0 + (size_t)NH * 1024 + ((ST) - 28) * 1024); \
                       XLOAD(ring[(ST) % R][2], wo0 + ((ST) - 28) * 1024); XLOAD(ring[(ST) % R][3], wo0 + (size_t)NH * 1024 + ((ST) - 28) * 1024); } \
    } while (0)
    f32x4 kvv[8][2];
    {
      const char* kb = ws + OFF_KV + ((size_t)(8 * w) * 272 + ((l & 31) < 16 ? (l & 31) * 16 : 256) + 4 * (l >> 5)) * 4;
#pragma unroll
      for (int hq = 0; hq < 8; ++hq) {
        asm volatile("global_load_dwordx4 %0, %1, off" : "=v"(kvv[hq][0]) : "v"(kb + hq * 272 * 4) : "memory");
        asm volatile("global_load_dwordx4 %0, %1, off" : "=v"(kvv[hq][1]) : "v"(kb + hq * 272 * 4 + 32) : "memory");
      }
    }
#pragma unroll
    for (int st = 0; st < R - 1; ++st) K2_ISSUE(st);
    static_for<0, NST>([&](auto ic) {
      constexpr int cs = decltype(ic)::value;
      if constexpr (cs + R - 1 < NST) K2_ISSUE(cs + R - 1);
      constexpr int ahead = (NST - 1 - cs) < (R - 1) ? (NST - 1 - cs) : (R - 1);
      s16x8* f = ring[cs % R];
      asm volatile("s_waitcnt vmcnt(%4)" : "+v"(f[0]), "+v"(f[1]), "+v"(f[2]), "+v"(f[3]) : "i"(4 * ahead) : "memory");
      if constexpr (cs < 28) {
        ag[0][0] = __builtin_amdgcn_mfma_f32_32x32x16_bf16(f[2], f[0], ag[0][0], 0, 0, 0);
        ag[0][1] = __builtin_amdgcn_mfma_f32_32x32x16_bf16(f[2], f[1], ag[0][1], 0, 0, 0);
        ag[1][0] = __builtin_amdgcn_mfma_f32_32x32x16_bf16(f[3], f[0], ag[1][0], 0, 0, 0);
        ag[1][1] = __builtin_amdgcn_mfma_f32_32x32x16_bf16(f[3], f[1], ag[1][1], 0, 0, 0);
      } else {
        asm volatile("" : "+v"(kvv[cs - 28][0]), "+v"(kvv[cs - 28][1]));
        u32x4 kp;
        kp[0] = pack2(kvv[cs - 28][0][0], kvv[cs - 28][0][1]); kp[1] = pack2(kvv[cs - 28][0][2], kvv[cs - 28][0][3]);
        kp[2] = pack2(kvv[cs - 28][1][0], kvv[cs - 28][1][1]); kp[3] = pack2(kvv[cs - 28][1][2], kvv[cs - 28][1][3]);
        const s16x8 kf = __builtin_bit_cast(s16x8, kp);
#pragma unroll
        for (int r = 0; r < 2; ++r) {
          f32x16 nd;
#pragma unroll
          for (int e = 0; e < 16; ++e) nd[e] = 0.f;
          nd = __builtin_amdgcn_mfma_f32_32x32x16_bf16(kf, f[r], nd, 0, 0, 0);
          const float rden = __builtin_amdgcn_rcpf(rbf(rbf(nd[8]) + 1e-6f));
          u32x4 ob;
#pragma unroll
          for (int j = 0; j < 4; ++j) {
            const unsigned np = pack2(nd[2 * j], nd[2 * j + 1]);
            ob[j] = pack2(unlo(np) * rden, unhi(np) * rden);
          }
          const s16x8 of = __builtin_bit_cast(s16x8, ob);
          as[0][r] = __builtin_amdgcn_mfma_f32_32x32x16_bf16(f[2], of, as[0][r], 0, 0, 0);
          as[1][r] = __builtin_amdgcn_mfma_f32_32x32x16_bf16(f[3], of, as[1][r], 0, 0, 0);
        }
      }
    });
  }
  __syncthreads();
#pragma unroll
  for (int c = 0; c < 2; ++c)
#pragma unroll
    for (int r = 0; r < 2; ++r)
#pragma unroll
      for (int qd = 0; qd < 4; ++qd) {
        f32x4 vg = {ag[c][r][4 * qd], ag[c][r][4 * qd + 1], ag[c][r][4 * qd + 2], ag[c][r][4 * qd + 3]};
        f32x4 vs = {as[c][r][4 * qd], as[c][r][4 * qd + 1], as[c][r][4 * qd + 2], as[c][r][4 * qd + 3]};
        const int lp = l ^ ((c * 8 + qd * 2 + (l >> 5)) & 15);
        *reinterpret_cast<f32x4*>(lds + ((((w * 8 + c * 2 + r) * 4 + qd) * 64 + lp) * 16)) = vg;
        *reinterpret_cast<f32x4*>(lds + ((((w * 8 + 4 + c * 2 + r) * 4 + qd) * 64 + lp) * 16)) = vs;
      }
  __syncthreads();
#pragma unroll
  for (int it = 0; it < 4; ++it) {
    const int Q = it * 256 + tid, row = Q >> 4, nq = Q & 15;
    const int r = row >> 5, s = row & 31, c = nq >> 3, hh = nq & 1, qd = (nq & 7) >> 1;
    f32x4 vg = {0.f, 0.f, 0.f, 0.f}, vs = {0.f, 0.f, 0.f, 0.f};
#pragma unroll
    for (int ww = 0; ww < 4; ++ww) {
      vg += *reinterpret_cast<const f32x4*>(lds + ((((ww * 8 + c * 2 + r) * 4 + qd) * 64 + hh * 32 + (s ^ ((c * 8 + qd * 2 + hh) & 15))) * 16));
      vs += *reinterpret_cast<const f32x4*>(lds + ((((ww * 8 + 4 + c * 2 + r) * 4 + qd) * 64 + hh * 32 + (s ^ ((c * 8 + qd * 2 + hh) & 15))) * 16));
    }
    const size_t go = (size_t)(64 * rb + row) * D_ + 64 * cb + 32 * c + 8 * qd + 4 * hh;
    const u32x2 xv = xpre[it];
    const float xs[4] = {__uint_as_float(xv[0] << 16), __uint_as_float(xv[0] & 0xffff0000u), __uint_as_float(xv[1] << 16), __uint_as_float(xv[1] & 0xffff0000u)};
    float res[4];
#pragma unroll
    for (int e = 0; e < 4; ++e) {
      const float gp = rbf(vg[e]);
      const float gs = rbf(gp * __builtin_amdgcn_rcpf(1.0f + __builtin_amdgcn_exp2f(-1.4426950408889634f * gp)));
      res[e] = xs[e] + rbf(rbf(vs[e]) * gs);
    }
    u32x2 ov = {pack2(res[0], res[1]), pack2(res[2], res[3])};
    *reinterpret_cast<u32x2*>(out + go) = ov;
  }
}

extern "C" void kernel_launch(void* const* d_in, const int* in_sizes, int n_in,
                              void* d_out, int out_size, void* d_ws, size_t ws_size,
                              hipStream_t stream) {
  const u16* x = (const u16*)d_in[0];
  const u16* wqkv = (const u16*)d_in[1];
  const u16* wo = (const u16*)d_in[2];
  const u16* wg = (const u16*)d_in[4];
  const u16* wu = (const u16*)d_in[5];
  const u16* wd = (const u16*)d_in[6];
  char* ws = (char*)d_ws;
  (void)hipFuncSetAttribute((const void*)k1_gemm, hipFuncAttributeMaxDynamicSharedMemorySize, K1_LDS);
  (void)hipFuncSetAttribute((const void*)k2_gemm, hipFuncAttributeMaxDynamicSharedMemorySize, K2_LDS);
  k0_prep<<<256, 512, 0, stream>>>(x, wd, wo, ws);
  k1_gemm<<<256, 512, K1_LDS, stream>>>(wqkv, wg, wu, ws);
  k2_gemm<<<256, 256, K2_LDS, stream>>>(x, ws, (u16*)d_out);
}
```
